```python
import torch

def _softmax(x, axis=-1):
    return torch.softmax(x, dim=axis)
def _gelu(x):
    return torch.nn.functional.gelu(x, approximate="tanh")
def _silu(x):
    return torch.nn.functional.silu(x)
def _sigmoid(x):
    return torch.sigmoid(x)
def _layernorm(x, axis=-1, eps=1e-5):
    m = x.mean(dim=axis, keepdim=True)
    v = x.var(dim=axis, keepdim=True, unbiased=False)
    return (x - m) / torch.sqrt(v + eps)
def _scatter_add(tgt, idx, src, axis=0):
    return tgt.clone().index_add_(axis, idx, src)
def _segment_sum(src, seg, n):
    out = torch.zeros((n,) + src.shape[1:], dtype=src.dtype)
    return out.index_add_(0, seg, src)
def _pad(x, pw):
    flat = tuple(v for pair in reversed(pw) for v in pair)
    return torch.nn.functional.pad(x, flat)

kernel_name = 'recurrent_scan__linear_kernel_hedgehog__dense_activation_silu__prompt'

def setup_inputs():

    _w = (lambda a: (lambda t: t.to(torch.bfloat16) if t.is_floating_point() else t)(torch.as_tensor(a)))
    def _A_setup():
        B, S, D, H = 1, 2048, 640, 32
        Dh = D // H
        return {
            "x": _w((lambda *s: torch.randn(*s, dtype=torch.bfloat16) * 0.05)(B, S, D)),
            "wqkv": _w((lambda *s: torch.randn(*s, dtype=torch.bfloat16) * 0.4)(D, 3 * D)),
            "w_phi": _w((lambda *s: torch.randn(*s, dtype=torch.bfloat16) * 0.4)(H, Dh, Dh) * (Dh ** -0.5)),
            "wo": _w((lambda *s: torch.randn(*s, dtype=torch.bfloat16) * 0.2)(D, D)),
        }
    _A_in = _A_setup()

    def _B_setup():
        B, S, D, F = 1, 2048, 640, 1728
        return {
            "x": _w((lambda *s: torch.randn(*s, dtype=torch.bfloat16) * 0.05)(B, S, D)),
            "w1": _w((lambda *s: torch.randn(*s, dtype=torch.bfloat16) * 0.4)(D, F) * (D ** -0.5)),
            "w2": _w((lambda *s: torch.randn(*s, dtype=torch.bfloat16) * 0.1)(F, D) * (F ** -0.5)),
        }
    _B_in = _B_setup()

    return {
        "A_x": _A_in["x"],
        "A_wqkv": _A_in["wqkv"],
        "A_w_phi": _A_in["w_phi"],
        "A_wo": _A_in["wo"],
        "B_x": _B_in["x"],
        "B_w1": _B_in["w1"],
        "B_w2": _B_in["w2"],
    }

def reference(A_x, A_wqkv, A_w_phi, A_wo, B_x, B_w1, B_w2):
    def _A_ref(x, wqkv, w_phi, wo):
        B, S, D = x.shape
        H = 32; Dh = D // H
        qkv = (lambda x, *d: x.permute(*d))((x @ wqkv).reshape(B, S, 3, H, Dh), 2, 0, 3, 1, 4)
        q, k, v = qkv[0], qkv[1], qkv[2]
        pq = torch.einsum("bhsd,hdf->bhsf", q, w_phi)
        pk = torch.einsum("bhsd,hdf->bhsf", k, w_phi)
        phi_q = torch.concatenate([_softmax(pq, axis=-1), _softmax(-pq, axis=-1)], axis=-1)
        phi_k = torch.concatenate([_softmax(pk, axis=-1), _softmax(-pk, axis=-1)], axis=-1)
        kv = torch.einsum("bhsf,bhsd->bhfd", phi_k, v)
        z  = torch.einsum("bhsf->bhf", phi_k)
        num = torch.einsum("bhqf,bhfd->bhqd", phi_q, kv)
        den = torch.einsum("bhqf,bhf->bhq", phi_q, z)[..., None] + 1e-6
        o = num / den
        return (lambda x, *d: x.permute(*d))(o, 0, 2, 1, 3).reshape(B, S, D) @ wo

    def _B_ref(x, w1, w2):
        h = torch.einsum("bsd,df->bsf", x, w1)
        h = _silu(h)
        out = torch.einsum("bsf,fd->bsd", h, w2)
        return out

    x = A_x
    xn = _layernorm(x, axis=-1)
    s = _A_ref(xn, A_wqkv, A_w_phi, A_wo)
    g = _silu(_B_ref(xn, B_w1, B_w2))
    return x + s * g
```

```cpp
#include <hip/hip_runtime.h>
#include <hip/hip_bf16.h>
#include <stdint.h>

typedef __attribute__((ext_vector_type(8))) __bf16 bf16x8;
typedef __attribute__((ext_vector_type(2))) __bf16 bf16x2;
typedef __attribute__((ext_vector_type(4))) short s16x4;
typedef __attribute__((ext_vector_type(4))) float f32x4;
typedef __attribute__((ext_vector_type(2))) float f32x2;
typedef __attribute__((ext_vector_type(4))) unsigned u32x4;
typedef __attribute__((ext_vector_type(2))) unsigned u32x2;
#define LDSP(T, off) ((__attribute__((address_space(3))) T*)(uintptr_t)(off))
#define DEV __device__ __forceinline__

constexpr int S_ = 2048, D_ = 640, F_ = 1728;
constexpr int RB = 187, NRB = 11, NCT = 23;
constexpr float LOG2E = 1.4426950408889634f;

constexpr size_t WS_H = 0;
constexpr size_t WS_PQ = WS_H + 128 * 54 * 1024;
constexpr size_t WS_SL = WS_PQ + 32 * 5 * 2048 * 16;
constexpr size_t WS_O = WS_SL + 11 * 32 * 40 * 24 * 4;

DEV unsigned pk2(float a, float b) {
  bf16x2 p; p[0] = (__bf16)a; p[1] = (__bf16)b;
  return __builtin_bit_cast(unsigned, p);
}
DEV float rbf(float a) { return (float)(__bf16)a; }
DEV bf16x8 tr_frag(unsigned a0, unsigned a1) {
  s16x4 lo = __builtin_amdgcn_ds_read_tr16_b64_v4i16(LDSP(s16x4, a0));
  s16x4 hi = __builtin_amdgcn_ds_read_tr16_b64_v4i16(LDSP(s16x4, a1));
  u32x2 l = __builtin_bit_cast(u32x2, lo), h = __builtin_bit_cast(u32x2, hi);
  u32x4 r = {l[0], l[1], h[0], h[1]};
  return __builtin_bit_cast(bf16x8, r);
}
DEV f32x4 mfma(bf16x8 a, bf16x8 b, f32x4 c) { return __builtin_amdgcn_mfma_f32_16x16x32_bf16(a, b, c, 0, 0, 0); }
DEV void glds(const char* sbase, unsigned voff, unsigned lds_off) {
  asm volatile("s_mov_b32 m0, %2\n\ts_nop 0\n\tglobal_load_lds_dwordx4 %0, %1" ::"v"(voff), "s"(sbase), "s"(lds_off) : "memory");
}
#define WAITV(n) asm volatile("s_waitcnt vmcnt(" #n ")" ::: "memory")
#define WAITL() asm volatile("s_waitcnt lgkmcnt(0)" ::: "memory")
#define BAR() __builtin_amdgcn_s_barrier()

constexpr int K1_XB = 192 * 128, K1_WB = 2 * 64 * 160, K1_STG = K1_XB + K1_WB;
constexpr int K1_RAW = 3 * K1_STG;
constexpr int K1_ZERO = K1_RAW + 4096;
constexpr int K1_ST = K1_ZERO + 64;
constexpr int K1_CS = K1_ST + 2 * 192 * 8;
constexpr int K1_LDS = K1_CS + 640;
constexpr int PKS = 352, VS = 288, K1_V = 192 * PKS;

__global__ __launch_bounds__(512, 2) void k1_kernel(const char* __restrict__ x, const char* __restrict__ wqkv,
                                                     const char* __restrict__ wphi, const char* __restrict__ w1,
                                                     char* __restrict__ ws) {
  extern __shared__ __attribute__((aligned(16))) char smem[];
  const unsigned sb = (unsigned)(uintptr_t)smem;
  const int tid = threadIdx.x, lane = tid & 63;
  const int wave = __builtin_amdgcn_readfirstlane(tid >> 6);
  const int g = wave & 3, u = wave >> 2;
  const int fr = lane & 15, q4 = lane >> 4;
  int rb, ct;
  {
    const int bid = blockIdx.x, xcd = bid & 7, idx = bid >> 3;
    const int t = (xcd < 5 ? xcd * 32 : 160 + (xcd - 5) * 31) + idx;
    if (t < 138) { ct = t / 6; rb = t - ct * 6; } else { const int t2 = t - 138; ct = t2 / 5; rb = 6 + t2 - ct * 5; }
  }
  const int row0 = rb * RB;
  const int nvalid = (rb == NRB - 1) ? (S_ - (NRB - 1) * RB) : RB;
  const char* ub[2]; int uld[2]; int ucmax[2]; int ffj[2];
  if (ct < 8) {
    ub[0] = wqkv + (640 + 80 * ct) * 2; ub[1] = wqkv + (1280 + 80 * ct) * 2; uld[0] = uld[1] = 3840; ffj[0] = ffj[1] = 0;
  } else if (ct < 16) {
    ub[0] = wqkv + (80 * (ct - 8)) * 2; uld[0] = 3840; ffj[0] = 0;
    ffj[1] = ct - 8; ub[1] = w1 + 80 * ffj[1] * 2; uld[1] = 3456;
  } else {
    ffj[0] = 8 + 2 * (ct - 16); ffj[1] = ffj[0] + 1;
    ub[0] = w1 + 80 * ffj[0] * 2; ub[1] = w1 + 80 * ffj[1] * 2; uld[0] = uld[1] = 3456;
  }
  ucmax[0] = 9; ucmax[1] = (ct == NCT - 1) ? 5 : 9;
  const int hg = ct & 7;

  unsigned xsrc[3], wsrc[3]; const char* wbs[3]; int wstep[3];
#pragma unroll
  for (int i = 0; i < 3; ++i) {
    const int px = wave + 8 * i;
    const int r = 8 * px + (lane >> 3);
    int grow = row0 + r; grow = grow < S_ ? grow : S_ - 1;
    xsrc[i] = grow * 1280 + (((lane & 7) ^ (lane >> 3)) << 4);
    int pw = wave + 8 * i; pw = pw < 20 ? pw : 19;
    const int un = pw >= 10 ? 1 : 0;
    const int id = (pw - 10 * un) * 64 + lane;
    const int rho = id / 10; int c = id - rho * 10;
    const int cm = un ? ucmax[1] : ucmax[0];
    c = c < cm ? c : cm;
    const int kl = (rho & 32) + 8 * ((rho >> 2) & 3) + 4 * ((rho >> 4) & 1) + (rho & 3);
    const int ld = un ? uld[1] : uld[0];
    wsrc[i] = kl * ld + c * 16;
    wbs[i] = un ? ub[1] : ub[0];
    wstep[i] = 64 * ld;
  }
  auto issue = [&](int s) {
    const unsigned base = sb + (s % 3) * K1_STG;
#pragma unroll
    for (int i = 0; i < 3; ++i) glds(x + s * 128, xsrc[i], base + (wave + 8 * i) * 1024);
    glds(wbs[0] + s * wstep[0], wsrc[0], base + K1_XB + wave * 1024);
    glds(wbs[1] + s * wstep[1], wsrc[1], base + K1_XB + (wave + 8) * 1024);
    if (wave < 4) glds(wbs[2] + s * wstep[2], wsrc[2], base + K1_XB + (wave + 16) * 1024);
  };
  if (wave >= 4) {
    int id = (wave - 4) * 64 + lane; id = id < 200 ? id : 199;
    glds(wphi + hg * 3200, id * 16, sb + K1_RAW + (wave - 4) * 1024);
  }
  issue(0); issue(1); issue(2);
  if (tid < 16) *LDSP(unsigned, sb + K1_ZERO + tid * 4) = 0u;

  unsigned xa[2], wa[2];
#pragma unroll
  for (int ss = 0; ss < 2; ++ss) {
    const int sub = ss ^ u;
    const int r = 48 * g + fr;
    xa[ss] = sb + r * 128 + (((4 * sub + q4) ^ (r & 7)) << 4);
    wa[ss] = sb + K1_XB + u * 10240 + (32 * sub + 4 * q4 + (fr >> 2)) * 160 + (fr & 3) * 8;
  }
  const bool ones_lane = (g == 3) && (fr == 15);
  f32x4 acc[3][5];
#pragma unroll
  for (int a = 0; a < 3; ++a)
#pragma unroll
    for (int t = 0; t < 5; ++t) acc[a][t] = f32x4{0.f, 0.f, 0.f, 0.f};
  float ssum[3] = {0.f, 0.f, 0.f}, ssq[3] = {0.f, 0.f, 0.f};
  const bf16x2 one2 = __builtin_bit_cast(bf16x2, 0x3F803F80u);

#pragma unroll
  for (int s = 0; s < 10; ++s) {
    if (s == 0) WAITV(10); else if (s < 9) WAITV(5); else WAITV(0);
    BAR();
    if (s >= 1 && s + 2 < 10) issue(s + 2);
    const unsigned so = (s % 3) * K1_STG;
#pragma unroll
    for (int ss = 0; ss < 2; ++ss) {
      u32x4 b[3];
#pragma unroll
      for (int a = 0; a < 3; ++a) b[a] = *LDSP(u32x4, xa[ss] + so + a * 2048);
      bf16x8 w[5];
#pragma unroll
      for (int t = 0; t < 5; ++t) w[t] = tr_frag(wa[ss] + so + t * 32, wa[ss] + so + t * 32 + 2560);
      if (ss == 0) {
#pragma unroll
        for (int a = 0; a < 3; ++a)
#pragma unroll
          for (int j = 0; j < 4; ++j) {
            const unsigned bw = b[a][j];
            const bf16x2 v = __builtin_bit_cast(bf16x2, bw);
            ssum[a] = __builtin_amdgcn_fdot2_f32_bf16(v, one2, ssum[a], false);
            ssq[a] = __builtin_amdgcn_fdot2_f32_bf16(v, v, ssq[a], false);
          }
      }
#pragma unroll
      for (int j = 0; j < 4; ++j) b[2][j] = ones_lane ? 0x3F803F80u : b[2][j];
#pragma unroll
      for (int t = 0; t < 5; ++t)
#pragma unroll
        for (int a = 0; a < 3; ++a) acc[a][t] = mfma(w[t], __builtin_bit_cast(bf16x8, b[a]), acc[a][t]);
    }
    WAITL();
  }
#pragma unroll
  for (int a = 0; a < 3; ++a) {
    ssum[a] += __shfl_xor(ssum[a], 16); ssum[a] += __shfl_xor(ssum[a], 32);
    ssq[a] += __shfl_xor(ssq[a], 16); ssq[a] += __shfl_xor(ssq[a], 32);
    if (q4 == 0) *LDSP(f32x2, sb + K1_ST + (u * 192 + 48 * g + 16 * a + fr) * 8) = f32x2{ssum[a], ssq[a]};
  }
  if (ones_lane) {
#pragma unroll
    for (int t = 0; t < 5; ++t) *LDSP(f32x4, sb + K1_CS + (u * 80 + 16 * t + 4 * q4) * 4) = acc[2][t];
  }
  __syncthreads();
  {
    f32x4 cs[5];
#pragma unroll
    for (int t = 0; t < 5; ++t) cs[t] = *LDSP(f32x4, sb + K1_CS + (u * 80 + 16 * t + 4 * q4) * 4);
#pragma unroll
    for (int a = 0; a < 3; ++a) {
      const int slot = 48 * g + 16 * a + fr;
      const f32x2 s0 = *LDSP(f32x2, sb + K1_ST + slot * 8), s1 = *LDSP(f32x2, sb + K1_ST + (192 + slot) * 8);
      float mean = (s0.x + s1.x) * (1.f / 640.f);
      float var = fmaxf((s0.y + s1.y) * (1.f / 640.f) - mean * mean, 0.f);
      const float rstd = 1.f / sqrtf(var + 1e-5f);
      const float mr = -mean * rstd;
#pragma unroll
      for (int t = 0; t < 5; ++t)
#pragma unroll
        for (int r = 0; r < 4; ++r) acc[a][t][r] = fmaf(acc[a][t][r], rstd, mr * cs[t][r]);
    }
  }
  const bool is_ffn = (ct >= 16) || (ct >= 8 && u == 1);
  if (is_ffn) {
    const int j = u ? ffj[1] : ffj[0];
    const int T0 = 5 * j;
    char* hb = ws + WS_H;
#pragma unroll
    for (int a = 0; a < 3; ++a) {
      const int slot = 48 * g + 16 * a + fr, tok = row0 + slot;
      u32x2 pk[5];
#pragma unroll
      for (int t = 0; t < 5; ++t) {
        float hv[4];
#pragma unroll
        for (int r = 0; r < 4; ++r) {
          const float y = acc[a][t][r];
          hv[r] = y * __builtin_amdgcn_rcpf(1.f + __builtin_amdgcn_exp2f(-LOG2E * y));
        }
        pk[t] = u32x2{pk2(hv[0], hv[1]), pk2(hv[2], hv[3])};
      }
      if (slot < nvalid) {
        char* dst = hb + ((size_t)(tok >> 4) * 54 * 64 + q4 * 16 + (tok & 15)) * 16;
        if (!(j & 1)) {
          const int ks = T0 >> 1;
          *(u32x4*)(dst + (size_t)ks * 1024) = u32x4{pk[0][0], pk[0][1], pk[1][0], pk[1][1]};
          *(u32x4*)(dst + (size_t)(ks + 1) * 1024) = u32x4{pk[2][0], pk[2][1], pk[3][0], pk[3][1]};
          *(u32x2*)(dst + (size_t)(ks + 2) * 1024) = pk[4];
        } else {
          const int ks = T0 >> 1;
          *(u32x2*)(dst + (size_t)ks * 1024 + 8) = pk[0];
          *(u32x4*)(dst + (size_t)(ks + 1) * 1024) = u32x4{pk[1][0], pk[1][1], pk[2][0], pk[2][1]};
          if (j != 21) *(u32x4*)(dst + (size_t)(ks + 2) * 1024) = u32x4{pk[3][0], pk[3][1], pk[4][0], pk[4][1]};
        }
      }
    }
  } else if (u == 0) {
    bf16x8 wf[5][3];
    {
      const int q = fr >> 2, p = fr & 3;
#pragma unroll
      for (int c = 0; c < 3; ++c) {
        const int ph0 = 32 * c + 4 * q4 + q, ph1 = ph0 + 16;
        const unsigned a0 = (ph0 / 20 == p) ? sb + K1_RAW + ph0 * 40 : sb + K1_ZERO;
        const unsigned a1 = (ph1 / 20 == p) ? sb + K1_RAW + ph1 * 40 : sb + K1_ZERO;
#pragma unroll
        for (int t = 0; t < 5; ++t) wf[t][c] = tr_frag(a0 + 8 * t, a1 + 8 * t);
      }
    }
#pragma unroll
    for (int a = 0; a < 3; ++a) {
      const int slot = 48 * g + 16 * a + fr, tok = row0 + slot;
      u32x4 qb[3];
      qb[0] = u32x4{pk2(acc[a][0][0], acc[a][0][1]), pk2(acc[a][0][2], acc[a][0][3]), pk2(acc[a][1][0], acc[a][1][1]), pk2(acc[a][1][2], acc[a][1][3])};
      qb[1] = u32x4{pk2(acc[a][2][0], acc[a][2][1]), pk2(acc[a][2][2], acc[a][2][3]), pk2(acc[a][3][0], acc[a][3][1]), pk2(acc[a][3][2], acc[a][3][3])};
      qb[2] = u32x4{pk2(acc[a][4][0], acc[a][4][1]), pk2(acc[a][4][2], acc[a][4][3]), 0u, 0u};
      f32x4 p[5];
#pragma unroll
      for (int t = 0; t < 5; ++t) {
        p[t] = f32x4{0.f, 0.f, 0.f, 0.f};
#pragma unroll
        for (int c = 0; c < 3; ++c) p[t] = mfma(wf[t][c], __builtin_bit_cast(bf16x8, qb[c]), p[t]);
      }
      float mx = p[0][0], mn = p[0][0];
#pragma unroll
      for (int t = 0; t < 5; ++t)
#pragma unroll
        for (int r = 0; r < 4; ++r) { mx = fmaxf(mx, p[t][r]); mn = fminf(mn, p[t][r]); }
      const float mxl = -mx * LOG2E, mnl = mn * LOG2E;
      float ep[20], en[20], sp = 0.f, sn = 0.f;
#pragma unroll
      for (int t = 0; t < 5; ++t)
#pragma unroll
        for (int r = 0; r < 4; ++r) {
          ep[4 * t + r] = __builtin_amdgcn_exp2f(fmaf(p[t][r], LOG2E, mxl));
          en[4 * t + r] = __builtin_amdgcn_exp2f(fmaf(p[t][r], -LOG2E, mnl));
          sp += ep[4 * t + r]; sn += en[4 * t + r];
        }
      float rp = __builtin_amdgcn_rcpf(sp), rn = __builtin_amdgcn_rcpf(sn);
      const bool valid = slot < nvalid;
      if (!valid) { rp = 0.f; rn = 0.f; }
      unsigned ph[20];
#pragma unroll
      for (int i = 0; i < 10; ++i) {
        ph[i] = pk2(ep[2 * i] * rp, ep[2 * i + 1] * rp);
        ph[10 + i] = pk2(en[2 * i] * rn, en[2 * i + 1] * rn);
      }
      if (ct >= 8) {
        if (valid) {
          char* dst = ws + WS_PQ + ((size_t)((4 * hg + q4) * 5) * 2048 + tok) * 16;
#pragma unroll
          for (int c = 0; c < 5; ++c)
            *(u32x4*)(dst + (size_t)c * 2048 * 16) = u32x4{ph[4 * c], ph[4 * c + 1], ph[4 * c + 2], ph[4 * c + 3]};
        }
      } else {
        if (!valid) {
#pragma unroll
          for (int i = 0; i < 20; ++i) ph[i] = 0u;
        }
#pragma unroll
        for (int c = 0; c < 5; ++c)
          *LDSP(u32x4, sb + slot * PKS + q4 * 80 + c * 16) = u32x4{ph[4 * c], ph[4 * c + 1], ph[4 * c + 2], ph[4 * c + 3]};
      }
    }
  } else {
#pragma unroll
    for (int a = 0; a < 3; ++a) {
      const int slot = 48 * g + 16 * a + fr;
      const bool valid = slot < nvalid;
#pragma unroll
      for (int t = 0; t < 5; ++t) {
        const int ft = 16 * t + 4 * q4, hd = ft / 20, d = ft - hd * 20;
        u32x2 v = u32x2{pk2(acc[a][t][0], acc[a][t][1]), pk2(acc[a][t][2], acc[a][t][3])};
        if (!valid) v = u32x2{0u, 0u};
        *LDSP(u32x2, sb + K1_V + slot * VS + hd * 64 + d * 2) = v;
      }
      *LDSP(u32x2, sb + K1_V + slot * VS + q4 * 64 + 40) = u32x2{0x3F80u, 0u};
    }
  }
  if (ct < 8) {
    __syncthreads();
    const int hd = wave & 3, rt = wave >> 2;
    const int rr = 4 * q4 + (fr >> 2);
    const unsigned va = sb + K1_V + rr * VS + hd * 64 + rt * 32 + (fr & 3) * 8;
    const unsigned pa = sb + rr * PKS + hd * 80 + (fr & 3) * 8;
    f32x4 kv[3];
#pragma unroll
    for (int c = 0; c < 3; ++c) kv[c] = f32x4{0.f, 0.f, 0.f, 0.f};
#pragma unroll
    for (int s = 0; s < 6; ++s) {
      const bf16x8 av = tr_frag(va + s * 32 * VS, va + s * 32 * VS + 16 * VS);
#pragma unroll
      for (int c = 0; c < 3; ++c) {
        const bf16x8 bp = tr_frag(pa + s * 32 * PKS + c * 32, pa + s * 32 * PKS + 16 * PKS + c * 32);
        kv[c] = mfma(av, bp, kv[c]);
      }
    }
    float* sl = (float*)(ws + WS_SL) + ((size_t)rb * 32 + 4 * hg + hd) * 960;
#pragma unroll
    for (int c = 0; c < 3; ++c) {
      const int f = 16 * c + fr, d = 16 * rt + 4 * q4;
      if (f < 40 && d < 24) *(f32x4*)(sl + f * 24 + d) = kv[c];
    }
  }
}

__global__ __launch_bounds__(256) void ko_kernel(char* __restrict__ ws) {
  __shared__ __attribute__((aligned(16))) char smem[64 * 96];
  const unsigned sb = (unsigned)(uintptr_t)smem;
  const int tid = threadIdx.x, lane = tid & 63, wave = tid >> 6;
  const int fr = lane & 15, q4 = lane >> 4;
  const int head = blockIdx.x >> 3, tg = blockIdx.x & 7;
  u32x4 b0[4], b1[4];
#pragma unroll
  for (int i = 0; i < 4; ++i) {
    const int tok = tg * 256 + wave * 64 + i * 16 + fr;
    const char* src = ws + WS_PQ + ((size_t)(head * 5) * 2048 + tok) * 16;
    b0[i] = *(const u32x4*)(src + (size_t)q4 * 2048 * 16);
    b1[i] = *(const u32x4*)(src + (size_t)4 * 2048 * 16);
  }
  if (tid < 240) {
    const float* sl = (const float*)(ws + WS_SL) + (size_t)head * 960 + tid * 4;
    f32x4 s = *(const f32x4*)sl;
#pragma unroll
    for (int r = 1; r < NRB; ++r) s += *(const f32x4*)(sl + (size_t)r * 32 * 960);
    const int f = tid / 6, c4 = tid - f * 6;
    const unsigned row = sb + f * 96;
    if (c4 < 4) *LDSP(u32x2, row + c4 * 8) = u32x2{pk2(s[0], s[1]), pk2(s[2], s[3])};
    else if (c4 == 4) {
#pragma unroll
      for (int p = 0; p < 4; ++p) *LDSP(unsigned short, row + 32 + p * 8) = (unsigned short)(pk2(s[p], 0.f) & 0xffffu);
    } else {
#pragma unroll
      for (int p = 0; p < 4; ++p) *LDSP(unsigned short, row + 32 + p * 8 + 2) = (unsigned short)(pk2(s[0], 0.f) & 0xffffu);
    }
  }
  if (tid < 24 * 4) *LDSP(u32x4, sb + (40 + (tid >> 2)) * 96 + (tid & 3) * 16) = u32x4{0u, 0u, 0u, 0u};
  __syncthreads();
  bf16x8 A[2][2];
  {
    const unsigned aa = sb + (8 * q4 + (fr >> 2)) * 96 + (fr & 3) * 8;
#pragma unroll
    for (int rt = 0; rt < 2; ++rt)
#pragma unroll
      for (int s = 0; s < 2; ++s) A[rt][s] = tr_frag(aa + s * 32 * 96 + rt * 32, aa + s * 32 * 96 + 4 * 96 + rt * 32);
  }
  const int G = head >> 3, h8 = head & 7;
#pragma unroll
  for (int i = 0; i < 4; ++i) {
    f32x4 c0 = {0.f, 0.f, 0.f, 0.f}, c1 = {0.f, 0.f, 0.f, 0.f};
    c0 = mfma(A[0][0], __builtin_bit_cast(bf16x8, b0[i]), c0);
    c0 = mfma(A[0][1], __builtin_bit_cast(bf16x8, b1[i]), c0);
    c1 = mfma(A[1][0], __builtin_bit_cast(bf16x8, b0[i]), c1);
    c1 = mfma(A[1][1], __builtin_bit_cast(bf16x8, b1[i]), c1);
    const float rd = 1.f / (c1[1] + 1e-6f);
    const int tt = tg * 16 + wave * 4 + i;
    char* dst = ws + WS_O + ((size_t)(tt * 20 + 5 * G) * 64 + lane) * 16;
    *(u32x2*)(dst + (h8 >> 1) * 1024 + (h8 & 1) * 8) = u32x2{pk2(c0[0] * rd, c0[1] * rd), pk2(c0[2] * rd, c0[3] * rd)};
    *(unsigned short*)(dst + 4 * 1024 + h8 * 2) = (unsigned short)(pk2(c1[0] * rd, 0.f) & 0xffffu);
  }
}

constexpr int K2_D = 4, K2_SLOT = 9 * 1024, K2_WAVE = K2_D * K2_SLOT, K2_LDS = 4 * K2_WAVE;

__global__ __launch_bounds__(256, 1) void k2_kernel(const char* __restrict__ x, const char* __restrict__ wo,
                                                     const char* __restrict__ w2, const char* __restrict__ ws,
                                                     char* __restrict__ out) {
  extern __shared__ __attribute__((aligned(16))) char smem[];
  const unsigned sb = (unsigned)(uintptr_t)smem;
  const int tid = threadIdx.x, lane = tid & 63;
  const int wave = __builtin_amdgcn_readfirstlane(tid >> 6);
  const int fr = lane & 15, q4 = lane >> 4;
  int tg, cg;
  {
    const int bid = blockIdx.x, xcd = bid & 7, idx = bid >> 3;
    tg = (xcd >> 1) * 8 + (idx & 7); cg = (xcd & 1) * 4 + (idx >> 3);
  }
  const bool isw = wave == 3;
  const int nsteps = isw ? 20 : 18;
  const char* asrc; int astride;
  if (isw) { asrc = ws + WS_O + (size_t)(tg * 4) * 20 * 1024; astride = 20 * 1024; }
  else { asrc = ws + WS_H + ((size_t)(tg * 4) * 54 + wave * 18) * 1024; astride = 54 * 1024; }
  const char* wbase = (isw ? wo : w2 + (size_t)wave * 18 * 32 * 1280) + cg * 160;
  int woffA[5], woffB[5];
#pragma unroll
  for (int i = 0; i < 5; ++i) {
    const int id = i * 64 + lane, rho = id / 10, c = id - rho * 10;
    const int ra = isw ? (rho < 16 ? rho : rho + 4) : rho;
    woffA[i] = ra * 1280 + c * 16;
    const int rbx = 20 * (4 * (rho >> 4) + (rho & 3)) + 16 + ((rho >> 2) & 3);
    woffB[i] = rbx * 1280 + c * 16;
  }
  const unsigned wb = sb + wave * K2_WAVE;
  auto issue = [&](int s) {
    const unsigned base = wb + (s % K2_D) * K2_SLOT;
#pragma unroll
    for (int a = 0; a < 4; ++a) glds(asrc + a * astride + s * 1024, lane * 16, base + a * 1024);
    int rowoff; bool ext = false;
    if (isw) { const int G = s / 5, r = s - G * 5; ext = (r == 4); rowoff = (160 * G + (ext ? 0 : 40 * r)) * 1280; }
    else rowoff = s * 32 * 1280;
#pragma unroll
    for (int i = 0; i < 5; ++i) glds(wbase + rowoff, ext ? woffB[i] : woffA[i], base + 4096 + i * 1024);
  };
#pragma unroll
  for (int s = 0; s < K2_D; ++s) issue(s);
  f32x4 acc[4][5];
#pragma unroll
  for (int a = 0; a < 4; ++a)
#pragma unroll
    for (int t = 0; t < 5; ++t) acc[a][t] = f32x4{0.f, 0.f, 0.f, 0.f};
  const unsigned ba = wb + lane * 16;
  const unsigned wa = wb + 4096 + (4 * q4 + (fr >> 2)) * 160 + (fr & 3) * 8;
  for (int s = 0; s < nsteps; ++s) {
    const int rem = nsteps - 1 - s;
    if (rem >= 3) WAITV(27); else if (rem == 2) WAITV(18); else if (rem == 1) WAITV(9); else WAITV(0);
    const unsigned so = (s % K2_D) * K2_SLOT;
    u32x4 b[4]; bf16x8 w[5];
#pragma unroll
    for (int a = 0; a < 4; ++a) b[a] = *LDSP(u32x4, ba + so + a * 1024);
#pragma unroll
    for (int t = 0; t < 5; ++t) w[t] = tr_frag(wa + so + t * 32, wa + so + t * 32 + 2560);
    WAITL();
    __builtin_amdgcn_sched_barrier(0);
    if (s + K2_D < nsteps) issue(s + K2_D);
#pragma unroll
    for (int t = 0; t < 5; ++t)
#pragma unroll
      for (int a = 0; a < 4; ++a) acc[a][t] = mfma(w[t], __builtin_bit_cast(bf16x8, b[a]), acc[a][t]);
  }
#pragma unroll
  for (int a = 0; a < 4; ++a)
    if (a != wave) {
#pragma unroll
      for (int t = 0; t < 5; ++t) *LDSP(f32x4, wb + (a * 5 + t) * 1024 + lane * 16) = acc[a][t];
    }
  const int tok = tg * 64 + wave * 16 + fr;
  const size_t ooff = (size_t)tok * 1280 + cg * 160 + q4 * 8;
  u32x2 xv[5];
#pragma unroll
  for (int t = 0; t < 5; ++t) xv[t] = *(const u32x2*)(x + ooff + t * 32);
  f32x4 own[5];
#pragma unroll
  for (int t = 0; t < 5; ++t) own[t] = wave == 0 ? acc[0][t] : wave == 1 ? acc[1][t] : wave == 2 ? acc[2][t] : acc[3][t];
  __syncthreads();
#pragma unroll
  for (int t = 0; t < 5; ++t) {
    f32x4 p[4];
#pragma unroll
    for (int w = 0; w < 4; ++w) {
      const f32x4 v = *LDSP(f32x4, sb + w * K2_WAVE + (wave * 5 + t) * 1024 + lane * 16);
      p[w] = (w == wave) ? own[t] : v;
    }
    const f32x4 B = p[0] + p[1] + p[2], sv = p[3];
    float o[4];
#pragma unroll
    for (int r = 0; r < 4; ++r) {
      const float xf = __builtin_bit_cast(float, (r & 1) ? (xv[t][r >> 1] & 0xffff0000u) : (xv[t][r >> 1] << 16));
      const float bb = B[r];
      const float gg = bb * __builtin_amdgcn_rcpf(1.f + __builtin_amdgcn_exp2f(-LOG2E * bb));
      o[r] = xf + sv[r] * gg;
    }
    *(u32x2*)(out + ooff + t * 32) = u32x2{pk2(o[0], o[1]), pk2(o[2], o[3])};
  }
}

extern "C" void kernel_launch(void* const* d_in, const int* in_sizes, int n_in, void* d_out, int out_size, void* d_ws,
                              size_t ws_size, hipStream_t stream) {
  const char* x = (const char*)d_in[0];
  const char* wqkv = (const char*)d_in[1];
  const char* wphi = (const char*)d_in[2];
  const char* wo = (const char*)d_in[3];
  const char* w1 = (const char*)d_in[5];
  const char* w2 = (const char*)d_in[6];
  char* ws = (char*)d_ws;
  (void)hipFuncSetAttribute((const void*)k1_kernel, hipFuncAttributeMaxDynamicSharedMemorySize, K1_LDS);
  (void)hipFuncSetAttribute((const void*)k2_kernel, hipFuncAttributeMaxDynamicSharedMemorySize, K2_LDS);
  k1_kernel<<<NRB * NCT, 512, K1_LDS, stream>>>(x, wqkv, wphi, w1, ws);
  ko_kernel<<<256, 256, 0, stream>>>(ws);
  k2_kernel<<<256, 256, K2_LDS, stream>>>(x, wo, w2, ws, (char*)d_out);
}
```

```cpp
#include <hip/hip_runtime.h>
#include <hip/hip_bf16.h>
#include <stdint.h>

typedef __attribute__((ext_vector_type(8))) __bf16 bf16x8;
typedef __attribute__((ext_vector_type(2))) __bf16 bf16x2;
typedef __attribute__((ext_vector_type(4))) short s16x4;
typedef __attribute__((ext_vector_type(4))) float f32x4;
typedef __attribute__((ext_vector_type(2))) float f32x2;
typedef __attribute__((ext_vector_type(4))) unsigned u32x4;
typedef __attribute__((ext_vector_type(2))) unsigned u32x2;
#define LDSP(T, off) ((__attribute__((address_space(3))) T*)(uintptr_t)(off))
#define DEV __device__ __forceinline__

constexpr int S_ = 2048, D_ = 640, F_ = 1728;
constexpr int RB = 187, NRB = 11, NCT = 23;
constexpr float LOG2E = 1.4426950408889634f;

constexpr size_t WS_H = 0;
constexpr size_t WS_PQ = WS_H + 128 * 54 * 1024;
constexpr size_t WS_SL = WS_PQ + 32 * 5 * 2048 * 16;
constexpr size_t WS_O = WS_SL + 11 * 32 * 40 * 24 * 4;
constexpr size_t WS_WF = WS_O + 128 * 20 * 1024;
constexpr size_t WS_CNT = WS_WF + 8 * 74 * 5120;

DEV unsigned pk2(float a, float b) {
  bf16x2 p; p[0] = (__bf16)a; p[1] = (__bf16)b;
  return __builtin_bit_cast(unsigned, p);
}
DEV float rbf(float a) { return (float)(__bf16)a; }
DEV bf16x8 tr_frag(unsigned a0, unsigned a1) {
  s16x4 lo = __builtin_amdgcn_ds_read_tr16_b64_v4i16(LDSP(s16x4, a0));
  s16x4 hi = __builtin_amdgcn_ds_read_tr16_b64_v4i16(LDSP(s16x4, a1));
  u32x2 l = __builtin_bit_cast(u32x2, lo), h = __builtin_bit_cast(u32x2, hi);
  u32x4 r = {l[0], l[1], h[0], h[1]};
  return __builtin_bit_cast(bf16x8, r);
}
DEV f32x4 mfma(bf16x8 a, bf16x8 b, f32x4 c) { return __builtin_amdgcn_mfma_f32_16x16x32_bf16(a, b, c, 0, 0, 0); }
DEV void glds(const char* sbase, unsigned voff, unsigned lds_off) {
  asm volatile("s_mov_b32 m0, %2\n\ts_nop 0\n\tglobal_load_lds_dwordx4 %0, %1" ::"v"(voff), "s"(sbase), "s"(lds_off) : "memory");
}
#define WAITV(n) asm volatile("s_waitcnt vmcnt(" #n ")" ::: "memory")
#define WAITL() __builtin_amdgcn_s_waitcnt(0xC07F)
#define BAR() asm volatile("s_barrier" ::: "memory")
#define SB() __builtin_amdgcn_sched_barrier(0)
#define CFENCE() asm volatile("" ::: "memory")
#define STAMP_DECL
#define STAMP(k, i)
#define STAMP_FLUSH(k)

constexpr int K1_XB = 192 * 128, K1_WB = 2 * 64 * 160, K1_STG = K1_XB + K1_WB;
constexpr int K1_RAW = 3 * K1_STG;
constexpr int K1_ZERO = K1_RAW + 4096;
constexpr int K1_ST = K1_ZERO + 64;
constexpr int K1_CS = K1_ST + 2 * 192 * 8;
constexpr int K1_RL = K1_CS + 640;
constexpr int K1_LDS = K1_RL + 3 * 5120;
constexpr int PKS = 352, VS = 288, K1_V = 192 * PKS;

__global__ __launch_bounds__(512, 2) void k1_kernel(const char* __restrict__ x, const char* __restrict__ wqkv,
                                                     const char* __restrict__ wphi, const char* __restrict__ w1,
                                                     const char* __restrict__ wo, const char* __restrict__ w2,
                                                     char* __restrict__ ws) {
  extern __shared__ __attribute__((aligned(16))) char smem[];
  const unsigned sb = (unsigned)(uintptr_t)smem;
  const int tid = threadIdx.x, lane = tid & 63;
  const int wave = __builtin_amdgcn_readfirstlane(tid >> 6);
  const int g = wave & 3, u = wave >> 2;
  const int fr = lane & 15, q4 = lane >> 4;
  STAMP_DECL; STAMP(0, 0);
  int rb, ct;
  {
    const int bid = blockIdx.x, xcd = bid & 7, idx = bid >> 3;
    const int t = (xcd < 5 ? xcd * 32 : 160 + (xcd - 5) * 31) + idx;
    if (t < 138) { ct = t / 6; rb = t - ct * 6; } else { const int t2 = t - 138; ct = t2 / 5; rb = 6 + t2 - ct * 5; }
  }
  const int rot = (3 * ct + 7 * rb) % 10;
  const int row0 = rb * RB;
  const int nvalid = (rb == NRB - 1) ? (S_ - (NRB - 1) * RB) : RB;
  const char* ub[2]; int uld[2]; int ucmax[2]; int ffj[2];
  if (ct < 8) {
    ub[0] = wqkv + (640 + 80 * ct) * 2; ub[1] = wqkv + (1280 + 80 * ct) * 2; uld[0] = uld[1] = 3840; ffj[0] = ffj[1] = 0;
  } else if (ct < 16) {
    ub[0] = wqkv + (80 * (ct - 8)) * 2; uld[0] = 3840; ffj[0] = 0;
    ffj[1] = ct - 8; ub[1] = w1 + 80 * ffj[1] * 2; uld[1] = 3456;
  } else {
    ffj[0] = 8 + 2 * (ct - 16); ffj[1] = ffj[0] + 1;
    ub[0] = w1 + 80 * ffj[0] * 2; ub[1] = w1 + 80 * ffj[1] * 2; uld[0] = uld[1] = 3456;
  }
  ucmax[0] = 9; ucmax[1] = (ct == NCT - 1) ? 5 : 9;
  const int hg = ct & 7;

  unsigned xsrc[3], wsrc[3]; const char* wbs[3]; int wstep[3];
#pragma unroll
  for (int i = 0; i < 3; ++i) {
    const int px = wave + 8 * i;
    const int r = 8 * px + (lane >> 3);
    int grow = row0 + r; grow = grow < S_ ? grow : S_ - 1;
    xsrc[i] = grow * 1280 + (((lane & 7) ^ (lane >> 3)) << 4);
    int pw = wave + 8 * i; pw = pw < 20 ? pw : 19;
    const int un = pw >= 10 ? 1 : 0;
    const int id = (pw - 10 * un) * 64 + lane;
    const int rho = id / 10; int c = id - rho * 10;
    const int cm = un ? ucmax[1] : ucmax[0];
    c = c < cm ? c : cm;
    const int kl = (rho & 32) + 8 * ((rho >> 2) & 3) + 4 * ((rho >> 4) & 1) + (rho & 3);
    const int ld = un ? uld[1] : uld[0];
    wsrc[i] = kl * ld + c * 16;
    wbs[i] = un ? ub[1] : ub[0];
    wstep[i] = 64 * ld;
  }
  auto piece = [&](int s, int p) {
    const unsigned base = sb + (s % 3) * K1_STG;
    int k = s + rot; k = k >= 10 ? k - 10 : k;
    if (p < 2) glds(x + k * 128, xsrc[p], base + (wave + 8 * p) * 1024);
    else if (p == 2) { if (tid < 504) glds(x + k * 128, xsrc[2], base + (wave + 16) * 1024); }
    else if (p < 5) glds(wbs[p - 3] + k * wstep[p - 3], wsrc[p - 3], base + K1_XB + (wave + 8 * (p - 3)) * 1024);
    else if (wave < 4) glds(wbs[2] + k * wstep[2], wsrc[2], base + K1_XB + (wave + 16) * 1024);
  };
#pragma unroll
  for (int p = 0; p < 6; ++p) piece(0, p);
  if (wave >= 4) {
    int id = (wave - 4) * 64 + lane; id = id < 200 ? id : 199;
    glds(wphi + hg * 3200, id * 16, sb + K1_RAW + (wave - 4) * 1024);
  }
#pragma unroll
  for (int p = 0; p < 6; ++p) piece(1, p);
#pragma unroll
  for (int p = 0; p < 6; ++p) piece(2, p);
  if (tid < 16) *LDSP(unsigned, sb + K1_ZERO + tid * 4) = 0u;
  if (tid >= 64 && tid < 88) {
    const int i = tid - 64;
    *LDSP(u32x4, sb + (i >> 3) * K1_STG + 191 * 128 + (i & 7) * 16) = u32x4{0x3F803F80u, 0x3F803F80u, 0x3F803F80u, 0x3F803F80u};
  }
  if (blockIdx.x == 0 && tid >= 128 && tid < 136) *(unsigned*)(ws + WS_CNT + (tid - 128) * 256) = 0u;
  const int rl_unit = (int)blockIdx.x + 253 * (wave - 4);
  const bool has_rl = (wave >= 4 && wave < 7 && rl_unit < 592);

  unsigned xa[2], wa[2], ga[3], ea;
#pragma unroll
  for (int ss = 0; ss < 2; ++ss) {
    const int sub = ss ^ u;
    const int r = 48 * g + fr;
    xa[ss] = sb + r * 128 + (((4 * sub + q4) ^ (r & 7)) << 4);
    wa[ss] = sb + K1_XB + u * 10240 + (32 * sub + 4 * q4 + (fr >> 2)) * 160 + (fr & 3) * 8;
  }
#pragma unroll
  for (int a = 0; a < 3; ++a) ga[a] = fr == 0 ? sb + 191 * 128 + q4 * 16 : xa[0] + a * 2048;
  ea = sb + (48 * g + 16 * (fr < 3 ? fr : 0)) * 128 + ((4 * u + q4) << 4);
  const bool ones_lane = (g == 3) && (fr == 15);
  f32x4 acc[3][5], gram[3], gramE = f32x4{0.f, 0.f, 0.f, 0.f};
#pragma unroll
  for (int a = 0; a < 3; ++a) {
    gram[a] = f32x4{0.f, 0.f, 0.f, 0.f};
#pragma unroll
    for (int t = 0; t < 5; ++t) acc[a][t] = f32x4{0.f, 0.f, 0.f, 0.f};
  }
  bf16x8 fb[2][3], fw[2][5], fg[3], fe;
  auto rd = [&](int s, int ss) {
    const unsigned so = (s % 3) * K1_STG;
#pragma unroll
    for (int a = 0; a < 3; ++a) fb[ss][a] = *LDSP(bf16x8, xa[ss] + so + a * 2048);
#pragma unroll
    for (int t = 0; t < 5; ++t) fw[ss][t] = tr_frag(wa[ss] + so + t * 32, wa[ss] + so + t * 32 + 2560);
    if (ss == 0) {
#pragma unroll
      for (int a = 0; a < 3; ++a) fg[a] = *LDSP(bf16x8, ga[a] + so);
      fe = *LDSP(bf16x8, ea + so);
    }
  };
  auto mm = [&](int ss, int t) {
#pragma unroll
    for (int a = 0; a < 3; ++a) acc[a][t] = mfma(fw[ss][t], fb[ss][a], acc[a][t]);
  };
  if (wave < 4) WAITV(12); else WAITV(11);
  WAITL();
  BAR();
  STAMP(0, 1);
  rd(0, 0);
#pragma unroll
  for (int s = 0; s < 10; ++s) {
    const bool iss0 = (s >= 1 && s + 2 < 10), iss1 = (s + 3 < 10);
    WAITL(); SB();
    rd(s, 1);
    mm(0, 0);
#pragma unroll
    for (int a = 0; a < 3; ++a) gram[a] = mfma(fg[a], fb[0][a], gram[a]);
    gramE = mfma(fe, fe, gramE);
    SB(); if (iss0) piece(s + 2, 3); SB();
    mm(0, 1); mm(0, 2);
    SB(); if (iss0) piece(s + 2, 4); SB();
    mm(0, 3);
    SB(); if (iss0) piece(s + 2, 5); SB();
    mm(0, 4);
    SB();
    WAITL();
    if (s < 9) {
      if (s < 8) { if (wave < 4) WAITV(6); else WAITV(5); }
      else { if (has_rl) WAITV(5); else WAITV(0); }
      BAR();
      if (s == 4) STAMP(0, 2);
      rd(s + 1, 0);
    }
    mm(1, 0); mm(1, 1);
    SB(); if (iss1) piece(s + 3, 0); SB();
    mm(1, 2);
    SB(); if (iss1) piece(s + 3, 1); SB();
    mm(1, 3);
    SB(); if (iss1) piece(s + 3, 2);
    if (s == 7 && has_rl) {
      const int cg = rl_unit / 74, ks = rl_unit - cg * 74;
      const char* src = (ks < 54 ? w2 + ks * 32 * 1280 : wo) + cg * 160;
#pragma unroll
      for (int i = 0; i < 5; ++i) {
        const int id = i * 64 + lane, rho = id / 10, c = id - rho * 10;
        int row = rho;
        if (ks >= 54) {
          const int ch = 4 * (ks - 54) + ((rho >> 2) & 3), P = ch / 5, r5 = ch - 5 * P, hi = rho >> 4, i4 = rho & 3;
          row = r5 < 4 ? 40 * P + 20 * (r5 >> 1) + 8 * (r5 & 1) + 4 * hi + i4 : 40 * P + 20 * hi + 16 + i4;
        }
        glds(src, row * 1280 + c * 16, sb + K1_RL + (wave - 4) * 5120 + i * 1024);
      }
    }
    SB();
    mm(1, 4);
    SB();
  }
  STAMP(0, 3);
  if (has_rl) {
    WAITV(0);
    const unsigned ra = sb + K1_RL + (wave - 4) * 5120 + (4 * q4 + (fr >> 2)) * 160 + (fr & 3) * 8;
    char* dst = ws + WS_WF + (size_t)rl_unit * 5120 + lane * 16;
#pragma unroll
    for (int t = 0; t < 5; ++t) *(bf16x8*)(dst + t * 1024) = tr_frag(ra + t * 32, ra + t * 32 + 2560);
  }
  {
    const int r3 = fr & 3;
#pragma unroll
    for (int a = 0; a < 3; ++a) {
      const unsigned o = sb + K1_ST + (u * 192 + 48 * g + 16 * a + fr) * 4;
      if (q4 == 0) *LDSP(float, o) = gram[a][0];
      const float dg = r3 == 0 ? gram[a][0] : r3 == 1 ? gram[a][1] : r3 == 2 ? gram[a][2] : gram[a][3];
      if (q4 == (fr >> 2) && fr != 0) *LDSP(float, o + 1536) = dg;
    }
    const float de = r3 == 0 ? gramE[0] : r3 == 1 ? gramE[1] : gramE[2];
    if (lane < 3) *LDSP(float, sb + K1_ST + 1536 + (u * 192 + 48 * g + 16 * lane) * 4) = de;
  }
  if (ones_lane) {
#pragma unroll
    for (int t = 0; t < 5; ++t) *LDSP(f32x4, sb + K1_CS + (u * 80 + 16 * t + 4 * q4) * 4) = acc[2][t];
  }
  WAITL(); BAR();
  {
    f32x4 cs[5];
#pragma unroll
    for (int t = 0; t < 5; ++t) cs[t] = *LDSP(f32x4, sb + K1_CS + (u * 80 + 16 * t + 4 * q4) * 4);
#pragma unroll
    for (int a = 0; a < 3; ++a) {
      const unsigned o = sb + K1_ST + (48 * g + 16 * a + fr) * 4;
      const float mean = (*LDSP(float, o) + *LDSP(float, o + 768)) * (1.f / 640.f);
      const float var = fmaxf((*LDSP(float, o + 1536) + *LDSP(float, o + 2304)) * (1.f / 640.f) - mean * mean, 0.f);
      const float rstd = 1.f / sqrtf(var + 1e-5f);
      const float mr = -mean * rstd;
#pragma unroll
      for (int t = 0; t < 5; ++t)
#pragma unroll
        for (int r = 0; r < 4; ++r) acc[a][t][r] = fmaf(acc[a][t][r], rstd, mr * cs[t][r]);
    }
  }
  const bool is_ffn = (ct >= 16) || (ct >= 8 && u == 1);
  if (is_ffn) {
    const int j = u ? ffj[1] : ffj[0];
    const int T0 = 5 * j;
    char* hb = ws + WS_H;
#pragma unroll
    for (int a = 0; a < 3; ++a) {
      const int slot = 48 * g + 16 * a + fr, tok = row0 + slot;
      u32x2 pk[5];
#pragma unroll
      for (int t = 0; t < 5; ++t) {
        float hv[4];
#pragma unroll
        for (int r = 0; r < 4; ++r) {
          const float y = acc[a][t][r];
          hv[r] = y * __builtin_amdgcn_rcpf(1.f + __builtin_amdgcn_exp2f(-LOG2E * y));
        }
        pk[t] = u32x2{pk2(hv[0], hv[1]), pk2(hv[2], hv[3])};
      }
      if (slot < nvalid) {
        char* dst = hb + ((size_t)(tok >> 4) * 54 * 64 + q4 * 16 + (tok & 15)) * 16;
        if (!(j & 1)) {
          const int ks = T0 >> 1;
          *(u32x4*)(dst + (size_t)ks * 1024) = u32x4{pk[0][0], pk[0][1], pk[1][0], pk[1][1]};
          *(u32x4*)(dst + (size_t)(ks + 1) * 1024) = u32x4{pk[2][0], pk[2][1], pk[3][0], pk[3][1]};
          *(u32x2*)(dst + (size_t)(ks + 2) * 1024) = pk[4];
        } else {
          const int ks = T0 >> 1;
          *(u32x2*)(dst + (size_t)ks * 1024 + 8) = pk[0];
          *(u32x4*)(dst + (size_t)(ks + 1) * 1024) = u32x4{pk[1][0], pk[1][1], pk[2][0], pk[2][1]};
          if (j != 21) *(u32x4*)(dst + (size_t)(ks + 2) * 1024) = u32x4{pk[3][0], pk[3][1], pk[4][0], pk[4][1]};
        }
      }
    }
  } else if (u == 0) {
    bf16x8 wf[5][3];
    {
      const int q = fr >> 2, p = fr & 3;
#pragma unroll
      for (int c = 0; c < 3; ++c) {
        const int ph0 = 32 * c + 4 * q4 + q, ph1 = ph0 + 16;
        const unsigned a0 = (ph0 / 20 == p) ? sb + K1_RAW + ph0 * 40 : sb + K1_ZERO;
        const unsigned a1 = (ph1 / 20 == p) ? sb + K1_RAW + ph1 * 40 : sb + K1_ZERO;
#pragma unroll
        for (int t = 0; t < 5; ++t) wf[t][c] = tr_frag(a0 + 8 * t, a1 + 8 * t);
      }
    }
#pragma unroll
    for (int a = 0; a < 3; ++a) {
      const int slot = 48 * g + 16 * a + fr, tok = row0 + slot;
      u32x4 qb[3];
      qb[0] = u32x4{pk2(acc[a][0][0], acc[a][0][1]), pk2(acc[a][0][2], acc[a][0][3]), pk2(acc[a][1][0], acc[a][1][1]), pk2(acc[a][1][2], acc[a][1][3])};
      qb[1] = u32x4{pk2(acc[a][2][0], acc[a][2][1]), pk2(acc[a][2][2], acc[a][2][3]), pk2(acc[a][3][0], acc[a][3][1]), pk2(acc[a][3][2], acc[a][3][3])};
      qb[2] = u32x4{pk2(acc[a][4][0], acc[a][4][1]), pk2(acc[a][4][2], acc[a][4][3]), 0u, 0u};
      f32x4 p[5];
#pragma unroll
      for (int t = 0; t < 5; ++t) {
        p[t] = f32x4{0.f, 0.f, 0.f, 0.f};
#pragma unroll
        for (int c = 0; c < 3; ++c) p[t] = mfma(wf[t][c], __builtin_bit_cast(bf16x8, qb[c]), p[t]);
      }
      float mx = p[0][0], mn = p[0][0];
#pragma unroll
      for (int t = 0; t < 5; ++t)
#pragma unroll
        for (int r = 0; r < 4; ++r) { mx = fmaxf(mx, p[t][r]); mn = fminf(mn, p[t][r]); }
      const float mxl = -mx * LOG2E, mnl = mn * LOG2E;
      float ep[20], en[20], sp = 0.f, sn = 0.f;
#pragma unroll
      for (int t = 0; t < 5; ++t)
#pragma unroll
        for (int r = 0; r < 4; ++r) {
          ep[4 * t + r] = __builtin_amdgcn_exp2f(fmaf(p[t][r], LOG2E, mxl));
          en[4 * t + r] = __builtin_amdgcn_exp2f(fmaf(p[t][r], -LOG2E, mnl));
          sp += ep[4 * t + r]; sn += en[4 * t + r];
        }
      float rp = __builtin_amdgcn_rcpf(sp), rn = __builtin_amdgcn_rcpf(sn);
      const bool valid = slot < nvalid;
      if (!valid) { rp = 0.f; rn = 0.f; }
      unsigned ph[20];
#pragma unroll
      for (int i = 0; i < 10; ++i) {
        ph[i] = pk2(ep[2 * i] * rp, ep[2 * i + 1] * rp);
        ph[10 + i] = pk2(en[2 * i] * rn, en[2 * i + 1] * rn);
      }
      if (ct >= 8) {
        if (valid) {
          char* dst = ws + WS_PQ + ((size_t)((4 * hg + q4) * 5) * 2048 + tok) * 16;
#pragma unroll
          for (int c = 0; c < 5; ++c)
            *(u32x4*)(dst + (size_t)c * 2048 * 16) = u32x4{ph[4 * c], ph[4 * c + 1], ph[4 * c + 2], ph[4 * c + 3]};
        }
      } else {
        if (!valid) {
#pragma unroll
          for (int i = 0; i < 20; ++i) ph[i] = 0u;
        }
#pragma unroll
        for (int c = 0; c < 5; ++c)
          *LDSP(u32x4, sb + slot * PKS + q4 * 80 + c * 16) = u32x4{ph[4 * c], ph[4 * c + 1], ph[4 * c + 2], ph[4 * c + 3]};
      }
    }
  } else {
#pragma unroll
    for (int a = 0; a < 3; ++a) {
      const int slot = 48 * g + 16 * a + fr;
      const bool valid = slot < nvalid;
#pragma unroll
      for (int t = 0; t < 5; ++t) {
        const int ft = 16 * t + 4 * q4, hd = ft / 20, d = ft - hd * 20;
        u32x2 v = u32x2{pk2(acc[a][t][0], acc[a][t][1]), pk2(acc[a][t][2], acc[a][t][3])};
        if (!valid) v = u32x2{0u, 0u};
        *LDSP(u32x2, sb + K1_V + slot * VS + hd * 64 + d * 2) = v;
      }
      *LDSP(u32x2, sb + K1_V + slot * VS + q4 * 64 + 40) = u32x2{0x3F80u, 0u};
    }
  }
  STAMP(0, 4);
  if (ct < 8) {
    WAITL(); BAR();
    const int hd = wave & 3, rt = wave >> 2;
    const int rr = 4 * q4 + (fr >> 2);
    const unsigned va = sb + K1_V + rr * VS + hd * 64 + rt * 32 + (fr & 3) * 8;
    const unsigned pa = sb + rr * PKS + hd * 80 + (fr & 3) * 8;
    f32x4 kv[3];
#pragma unroll
    for (int c = 0; c < 3; ++c) kv[c] = f32x4{0.f, 0.f, 0.f, 0.f};
#pragma unroll
    for (int s = 0; s < 6; ++s) {
      const bf16x8 av = tr_frag(va + s * 32 * VS, va + s * 32 * VS + 16 * VS);
#pragma unroll
      for (int c = 0; c < 3; ++c) {
        const bf16x8 bp = tr_frag(pa + s * 32 * PKS + c * 32, pa + s * 32 * PKS + 16 * PKS + c * 32);
        kv[c] = mfma(av, bp, kv[c]);
      }
    }
    float* sl = (float*)(ws + WS_SL) + ((size_t)rb * 32 + 4 * hg + hd) * 960;
#pragma unroll
    for (int c = 0; c < 3; ++c) {
      const int f = 16 * c + fr, d = 16 * rt + 4 * q4;
      if (f < 40 && d < 24) *(f32x4*)(sl + f * 24 + d) = kv[c];
    }
  }
  STAMP(0, 5); STAMP_FLUSH(0);
}

constexpr int K2_D = 4;
constexpr int K2_R1 = 0, K2_R2 = 61440, K2_KV = 122880, K2_CTR = K2_KV + 64 * 96, K2_LDS = K2_CTR + 16;
typedef __attribute__((address_space(1))) unsigned gu32;

__global__ __launch_bounds__(256, 1) void k2_kernel(const char* __restrict__ x, char* __restrict__ ws,
                                                     char* __restrict__ out) {
  extern __shared__ __attribute__((aligned(16))) char smem[];
  const unsigned sb = (unsigned)(uintptr_t)smem;
  const int tid = threadIdx.x, lane = tid & 63;
  const int wave = __builtin_amdgcn_readfirstlane(tid >> 6);
  const int fr = lane & 15, q4 = lane >> 4;
  STAMP_DECL; STAMP(1, 0);
  const int head = blockIdx.x >> 3, tq = blockIdx.x & 7;
  int tg, cg;
  {
    const int bid = blockIdx.x, xcd = bid & 7, idx = bid >> 3;
    tg = (xcd >> 1) * 8 + (idx & 7); cg = (xcd & 1) * 4 + (idx >> 3);
  }
  f32x4 sl[NRB];
  {
    const int t2 = tid < 240 ? tid : 239;
    const float* p = (const float*)(ws + WS_SL) + (size_t)head * 960 + t2 * 4;
#pragma unroll
    for (int r = 0; r < NRB; ++r) sl[r] = *(const f32x4*)(p + (size_t)r * 32 * 960);
  }
  bf16x8 b0[4], b1[4];
#pragma unroll
  for (int i = 0; i < 4; ++i) {
    const int tok = tq * 256 + wave * 64 + i * 16 + fr;
    const char* src = ws + WS_PQ + ((size_t)(head * 5) * 2048 + tok) * 16;
    b0[i] = *(const bf16x8*)(src + (size_t)q4 * 2048 * 16);
    b1[i] = *(const bf16x8*)(src + (size_t)4 * 2048 * 16);
  }
  CFENCE();
  const __amdgpu_buffer_rsrc_t rs = __builtin_amdgcn_make_buffer_rsrc((void*)ws, 0, (int)WS_CNT, 0x00020000);
  const unsigned l16 = lane * 16;
  const unsigned hsrc = (unsigned)WS_H + tg * 4 * 54 * 1024, wsrc = (unsigned)WS_WF + cg * 74 * 5120;
  bf16x8 ra[K2_D][4], rw[K2_D][5], ea[4], ew[5];
  auto ldh = [&](bf16x8 (&d)[4], int ks) {
#pragma unroll
    for (int a = 0; a < 4; ++a)
      d[a] = __builtin_bit_cast(bf16x8, __builtin_amdgcn_raw_buffer_load_b128(rs, l16, hsrc + (((wave + a) & 3) * 54 + ks) * 1024, 0));
  };
  auto ldw = [&](bf16x8 (&d)[5], int ks) {
#pragma unroll
    for (int t = 0; t < 5; ++t)
      d[t] = __builtin_bit_cast(bf16x8, __builtin_amdgcn_raw_buffer_load_b128(rs, l16 + (t & 3) * 1024, wsrc + ks * 5120 + (t >> 2) * 4096, 0));
  };
  const int eks = 52 + (wave & 1);
  if (wave < 2) { ldh(ea, eks); ldw(ew, eks); }
#pragma unroll
  for (int s = 0; s < K2_D; ++s) { ldh(ra[s], 13 * wave + s); ldw(rw[s], 13 * wave + s); }
  const int tok = tg * 64 + wave * 16 + fr;
  const size_t ooff = (size_t)tok * 1280 + cg * 160 + q4 * 8;
  u32x2 xv[5];
#pragma unroll
  for (int t = 0; t < 5; ++t) xv[t] = *(const u32x2*)(x + ooff + t * 32);
  CFENCE();
  if (tid < 24 * 6) *LDSP(u32x4, sb + K2_KV + 40 * 96 + tid * 16) = u32x4{0u, 0u, 0u, 0u};
  if (tid == 255) *LDSP(unsigned, sb + K2_CTR) = 0u;
  {
    f32x4 s = sl[0];
#pragma unroll
    for (int r = 1; r < NRB; ++r) s += sl[r];
    const int f = tid / 6, c4 = tid - f * 6;
    const unsigned row = sb + K2_KV + f * 96;
    const u32x2 v = u32x2{pk2(s[0], s[1]), pk2(s[2], s[3])};
    if (tid < 240) {
      if (c4 < 5) *LDSP(u32x2, row + (c4 & 1) * 32 + (c4 >> 1) * 8) = v;
      else {
#pragma unroll
        for (int p = 0; p < 4; ++p) *LDSP(u32x2, row + 64 + p * 8) = v;
      }
    }
  }
  STAMP(1, 1);
  WAITL(); BAR();
  {
    bf16x8 A[3][2];
    const unsigned aa = sb + K2_KV + (8 * q4 + (fr >> 2)) * 96 + (fr & 3) * 8;
#pragma unroll
    for (int rt = 0; rt < 3; ++rt)
#pragma unroll
      for (int s = 0; s < 2; ++s) A[rt][s] = tr_frag(aa + s * 32 * 96 + rt * 32, aa + s * 32 * 96 + 4 * 96 + rt * 32);
    const int P = head >> 1, hb = head & 1;
    const int chunk = q4 < 2 ? 5 * P + 2 * hb + q4 : 5 * P + 4;
    const unsigned ob = (unsigned)(chunk * 256 + fr * 16 + (q4 < 2 ? 0 : hb * 8));
    const unsigned os = (unsigned)WS_O + (tq * 16 + wave * 4) * 20480;
#pragma unroll
    for (int i = 0; i < 4; ++i) {
      f32x4 c[3];
#pragma unroll
      for (int rt = 0; rt < 3; ++rt) {
        c[rt] = mfma(A[rt][0], b0[i], f32x4{0.f, 0.f, 0.f, 0.f});
        c[rt] = mfma(A[rt][1], b1[i], c[rt]);
      }
      const float rd = 1.f / (c[2][0] + 1e-6f);
      const u32x4 v = u32x4{pk2(c[0][0] * rd, c[0][1] * rd), pk2(c[0][2] * rd, c[0][3] * rd),
                            pk2(c[1][0] * rd, c[1][1] * rd), pk2(c[1][2] * rd, c[1][3] * rd)};
      if (q4 < 2) __builtin_amdgcn_raw_buffer_store_b128(v, rs, ob, os + i * 20480, 16);
      else if (q4 == 2) __builtin_amdgcn_raw_buffer_store_b64(u32x2{v[0], v[1]}, rs, ob, os + i * 20480, 16);
    }
  }
  CFENCE();
  STAMP(1, 2);
  f32x4 acc[4][5];
#pragma unroll
  for (int a = 0; a < 4; ++a)
#pragma unroll
    for (int t = 0; t < 5; ++t) acc[a][t] = f32x4{0.f, 0.f, 0.f, 0.f};
  auto mm = [&](bf16x8 (&A)[4], bf16x8 (&W)[5]) {
#pragma unroll
    for (int t = 0; t < 5; ++t)
#pragma unroll
      for (int a = 0; a < 4; ++a) acc[a][t] = mfma(W[t], A[a], acc[a][t]);
  };
  gu32* cnt_out = (gu32*)(ws + WS_CNT + tq * 256);
  gu32* cnt_in = (gu32*)(ws + WS_CNT + (tg >> 2) * 256);
  if (wave < 2) mm(ea, ew);
  unsigned seen = 0;
#pragma unroll
  for (int s = 0; s < 13; ++s) {
    const int d = s % K2_D;
    mm(ra[d], rw[d]);
    SB();
    if (s + K2_D < 13) { ldh(ra[d], 13 * wave + s + K2_D); ldw(rw[d], 13 * wave + s + K2_D); }
    else ldw(rw[d], 54 + 5 * wave + (s + K2_D - 13));
    if (s == 1) {
      WAITV(18);
      if (lane == 0) {
        const unsigned old = __hip_atomic_fetch_add(LDSP(unsigned, sb + K2_CTR), 1u, __ATOMIC_RELAXED, __HIP_MEMORY_SCOPE_WORKGROUP);
        if (old == 3u) __hip_atomic_fetch_add(cnt_out, 1u, __ATOMIC_RELAXED, __HIP_MEMORY_SCOPE_AGENT);
      }
      CFENCE();
    }
    if (s == 8) ldw(ew, 54 + 5 * wave + 4);
    if (s == 9) seen = __hip_atomic_load(cnt_in, __ATOMIC_RELAXED, __HIP_MEMORY_SCOPE_AGENT);
    SB();
  }
  STAMP(1, 3);
  {
    unsigned v = __builtin_amdgcn_readfirstlane(seen);
    for (int spin = 0; v < 32u && spin < (1 << 22); ++spin) {
      __builtin_amdgcn_s_sleep(2);
      v = __builtin_amdgcn_readfirstlane(__hip_atomic_load(cnt_in, __ATOMIC_RELAXED, __HIP_MEMORY_SCOPE_AGENT));
    }
    __builtin_amdgcn_fence(__ATOMIC_ACQUIRE, "wavefront");
    CFENCE();
  }
  STAMP(1, 4);
  bf16x8 oa[5][4];
  {
    const unsigned ob = (unsigned)WS_O + (tg * 4 * 20 + 5 * wave) * 1024;
#pragma unroll
    for (int s = 0; s < 5; ++s)
#pragma unroll
      for (int a = 0; a < 4; ++a)
        oa[s][a] = __builtin_bit_cast(bf16x8, __builtin_amdgcn_raw_buffer_load_b128(rs, l16 + (s & 3) * 1024, ob + ((wave + a) & 3) * 20480 + (s >> 2) * 4096, 16));
  }
  CFENCE(); SB();
  auto scatter = [&](unsigned reg) {
#pragma unroll
    for (int a = 1; a < 4; ++a) {
      const int slot = (((wave + a) & 3) * 3 + (a - 1)) * 5;
#pragma unroll
      for (int t = 0; t < 5; ++t) *LDSP(f32x4, reg + (slot + t) * 1024 + lane * 16) = acc[a][t];
    }
  };
  auto gather = [&](unsigned reg, f32x4 (&r)[5]) {
    WAITL(); BAR();
#pragma unroll
    for (int t = 0; t < 5; ++t) r[t] = acc[0][t];
#pragma unroll
    for (int j = 0; j < 3; ++j)
#pragma unroll
      for (int t = 0; t < 5; ++t) r[t] += *LDSP(f32x4, reg + ((wave * 3 + j) * 5 + t) * 1024 + lane * 16);
  };
  f32x4 gv[5];
  scatter(sb + K2_R1);
  gather(sb + K2_R1, gv);
#pragma unroll
  for (int t = 0; t < 5; ++t)
#pragma unroll
    for (int r = 0; r < 4; ++r) {
      const float bb = gv[t][r];
      gv[t][r] = bb * __builtin_amdgcn_rcpf(1.f + __builtin_amdgcn_exp2f(-LOG2E * bb));
    }
  SB();
  STAMP(1, 5);
#pragma unroll
  for (int a = 0; a < 4; ++a)
#pragma unroll
    for (int t = 0; t < 5; ++t) acc[a][t] = f32x4{0.f, 0.f, 0.f, 0.f};
#pragma unroll
  for (int s = 0; s < 4; ++s) mm(oa[s], rw[(13 - K2_D + s) % K2_D]);
  mm(oa[4], ew);
  STAMP(1, 6);
  f32x4 sv[5];
  scatter(sb + K2_R2);
  gather(sb + K2_R2, sv);
#pragma unroll
  for (int t = 0; t < 5; ++t) {
    float o[4];
#pragma unroll
    for (int r = 0; r < 4; ++r) {
      const unsigned xw = xv[t][r >> 1];
      const float xf = __builtin_bit_cast(float, (r & 1) ? (xw & 0xffff0000u) : (xw << 16));
      o[r] = fmaf(sv[t][r], gv[t][r], xf);
    }
    *(u32x2*)(out + ooff + t * 32) = u32x2{pk2(o[0], o[1]), pk2(o[2], o[3])};
  }
  STAMP(1, 7); STAMP_FLUSH(1);
}

extern "C" void kernel_launch(void* const* d_in, const int* in_sizes, int n_in, void* d_out, int out_size, void* d_ws,
                              size_t ws_size, hipStream_t stream) {
  const char* x = (const char*)d_in[0];
  const char* wqkv = (const char*)d_in[1];
  const char* wphi = (const char*)d_in[2];
  const char* wo = (const char*)d_in[3];
  const char* w1 = (const char*)d_in[5];
  const char* w2 = (const char*)d_in[6];
  char* ws = (char*)d_ws;
  (void)hipFuncSetAttribute((const void*)k1_kernel, hipFuncAttributeMaxDynamicSharedMemorySize, K1_LDS);
  (void)hipFuncSetAttribute((const void*)k2_kernel, hipFuncAttributeMaxDynamicSharedMemorySize, K2_LDS);
  k1_kernel<<<NRB * NCT, 512, K1_LDS, stream>>>(x, wqkv, wphi, w1, wo, w2, ws);
  k2_kernel<<<256, 256, K2_LDS, stream>>>(x, ws, (char*)d_out);
}
```

```cpp
#define ST_AUX 16
#define KV_AUX 0
#define NOROT
#include <hip/hip_runtime.h>
#include <hip/hip_bf16.h>
#include <stdint.h>

typedef __attribute__((ext_vector_type(8))) __bf16 bf16x8;
typedef __attribute__((ext_vector_type(2))) __bf16 bf16x2;
typedef __attribute__((ext_vector_type(4))) short s16x4;
typedef __attribute__((ext_vector_type(4))) float f32x4;
typedef __attribute__((ext_vector_type(2))) float f32x2;
typedef __attribute__((ext_vector_type(4))) unsigned u32x4;
typedef __attribute__((ext_vector_type(2))) unsigned u32x2;
#define LDSP(T, off) ((__attribute__((address_space(3))) T*)(uintptr_t)(off))
#define DEV __device__ __forceinline__

constexpr int S_ = 2048, D_ = 640, F_ = 1728;
constexpr int RB = 187, NRB = 11, NCT = 23;
constexpr float LOG2E = 1.4426950408889634f;

constexpr size_t WS_H = 0;
constexpr size_t WS_PQ = WS_H + 128 * 54 * 1024;
constexpr size_t WS_SL = WS_PQ + 32 * 2048 * 40;
constexpr size_t WS_O = WS_SL + 11 * 32 * 40 * 24 * 4;
constexpr size_t WS_WF = WS_O + 128 * 20 * 1024;
constexpr size_t WS_CNT = WS_WF + 8 * 74 * 5120;

DEV unsigned pk2(float a, float b) {
  bf16x2 p; p[0] = (__bf16)a; p[1] = (__bf16)b;
  return __builtin_bit_cast(unsigned, p);
}
DEV float rbf(float a) { return (float)(__bf16)a; }
DEV bf16x8 tr_frag(unsigned a0, unsigned a1) {
  s16x4 lo = __builtin_amdgcn_ds_read_tr16_b64_v4i16(LDSP(s16x4, a0));
  s16x4 hi = __builtin_amdgcn_ds_read_tr16_b64_v4i16(LDSP(s16x4, a1));
  u32x2 l = __builtin_bit_cast(u32x2, lo), h = __builtin_bit_cast(u32x2, hi);
  u32x4 r = {l[0], l[1], h[0], h[1]};
  return __builtin_bit_cast(bf16x8, r);
}
template <int N> struct IC { static constexpr int v = N; };
DEV u32x2 tr_half(unsigned a0) { return __builtin_bit_cast(u32x2, __builtin_amdgcn_ds_read_tr16_b64_v4i16(LDSP(s16x4, a0))); }
DEV bf16x8 cat(u32x2 l, u32x2 h) { u32x4 r = {l[0], l[1], h[0], h[1]}; return __builtin_bit_cast(bf16x8, r); }
DEV f32x2 lo2(f32x4 v) { return f32x2{v[0], v[1]}; }
DEV f32x2 hi2(f32x4 v) { return f32x2{v[2], v[3]}; }
DEV f32x2 fma2(f32x2 a, f32x2 b, f32x2 c) { return __builtin_elementwise_fma(a, b, c); }
DEV float max3(float a, float b, float c) { float r; asm("v_max3_f32 %0, %1, %2, %3" : "=v"(r) : "v"(a), "v"(b), "v"(c)); return r; }
DEV float min3(float a, float b, float c) { float r; asm("v_min3_f32 %0, %1, %2, %3" : "=v"(r) : "v"(a), "v"(b), "v"(c)); return r; }
DEV f32x2 sp2(float a) { return f32x2{a, a}; }
DEV unsigned pk2v(f32x2 v) { return pk2(v[0], v[1]); }
template <int IMM>
DEV void blds(__amdgpu_buffer_rsrc_t rs, unsigned voff, int k, int step, unsigned mbase) {
  int tmp;
  asm volatile("s_add_u32 m0, %5, %6\n\ts_mul_i32 %0, %3, %4\n\tbuffer_load_dwordx4 %1, %2, %0 offen lds"
               : "=&s"(tmp) : "v"(voff), "s"(rs), "s"(k), "s"(step), "s"(mbase), "i"(IMM) : "memory", "scc");
}
DEV __amdgpu_buffer_rsrc_t mkrs(char* ws, unsigned off) {
  return __builtin_amdgcn_make_buffer_rsrc((void*)(ws + off), 0, (int)((unsigned)WS_CNT - off), 0x00020000);
}
DEV f32x4 mfma(bf16x8 a, bf16x8 b, f32x4 c) { return __builtin_amdgcn_mfma_f32_16x16x32_bf16(a, b, c, 0, 0, 0); }
DEV void glds(const char* sbase, unsigned voff, unsigned lds_off) {
  asm volatile("s_mov_b32 m0, %2\n\ts_nop 0\n\tglobal_load_lds_dwordx4 %0, %1" ::"v"(voff), "s"(sbase), "s"(lds_off) : "memory");
}
#define WAITV(n) asm volatile("s_waitcnt vmcnt(" #n ")" ::: "memory")
#define WAITL() __builtin_amdgcn_s_waitcnt(0xC07F)
#define BAR() asm volatile("s_barrier" ::: "memory")
#define SB() __builtin_amdgcn_sched_barrier(0)
#define CFENCE() asm volatile("" ::: "memory")
#define STAMP_DECL
#define STAMP(k, i)
#define STAMP_FLUSH(k)

constexpr int K1_XB = 192 * 128, K1_WB = 2 * 64 * 160, K1_ONE = K1_XB + K1_WB, K1_STG = K1_ONE + 128;
constexpr int K1_RAW = 3 * K1_STG;
constexpr int K1_ZERO = K1_RAW + 4096;
constexpr int K1_ST = K1_ZERO + 64;
constexpr int K1_CS = K1_ST + 2 * 192 * 8;
constexpr int K1_RL = K1_STG;
constexpr int K1_LDS = K1_CS + 640;
constexpr int PKS = 352, VS = 288, K1_V = 192 * PKS;
#define RL_AUX 16

__global__ __launch_bounds__(512, 2) void k1_kernel(const char* __restrict__ x, const char* __restrict__ wqkv,
                                                     const char* __restrict__ wphi, const char* __restrict__ w1,
                                                     const char* __restrict__ wo, const char* __restrict__ w2,
                                                     char* __restrict__ ws) {
  extern __shared__ __attribute__((aligned(16))) char smem[];
  const unsigned sb = (unsigned)(uintptr_t)smem;
  const int tid = threadIdx.x, lane = tid & 63;
  const int wave = __builtin_amdgcn_readfirstlane(tid >> 6);
  const int g = wave & 3, u = wave >> 2;
  const int fr = lane & 15, q4 = lane >> 4;
  STAMP_DECL; STAMP(0, 0);
  unsigned kd;
  asm volatile("s_load_dword %0, %1, 0x0" : "=s"(kd) : "s"(__builtin_amdgcn_kernarg_segment_ptr()));
  if (wave == 7 && blockIdx.x < 32) {
    unsigned long long pc; int len;
    asm volatile("s_getpc_b64 %0\n.Lpf_here:\n\ts_mov_b32 %1, .Lpf_end-.Lpf_here" : "=s"(pc), "=s"(len));
    const __amdgpu_buffer_rsrc_t rc = __builtin_amdgcn_make_buffer_rsrc((void*)pc, 0, len - 4, 0x00020000);
#pragma unroll
    for (int i = 0; i < 5; ++i)
      asm volatile("s_mov_b32 m0, %2\n\ts_nop 0\n\tbuffer_load_dword %0, %1, 0 offen lds"
                   ::"v"(lane * 128 + i * 8192), "s"(rc), "s"(sb + K1_ST) : "memory");
  }
  int rb, ct;
  {
    const int bid = blockIdx.x, xcd = bid & 7, idx = bid >> 3;
    const int t = xcd * 32 - (xcd > 5 ? xcd - 5 : 0) + idx;
    const unsigned t2 = t < 138 ? 0u : (unsigned)(t - 138);
    const int c6 = (int)((unsigned)t / 6u), c5 = (int)(t2 / 5u);
    ct = t < 138 ? c6 : c5;
    rb = t < 138 ? t - c6 * 6 : 6 + (int)t2 - c5 * 5;
  }
  const int rot = 0;
  const int row0 = rb * RB;
  const int nvalid = (rb == NRB - 1) ? (S_ - (NRB - 1) * RB) : RB;
  const unsigned mb = sb + wave * 1024;
  const __amdgpu_buffer_rsrc_t rsx = __builtin_amdgcn_make_buffer_rsrc((void*)x, 0, S_ * D_ * 2, 0x00020000);
  unsigned xsrc[3];
#pragma unroll
  for (int i = 0; i < 3; ++i) {
    int grow = row0 + 8 * (wave + 8 * i) + (lane >> 3); grow = grow < S_ ? grow : S_ - 1;
    xsrc[i] = grow * 1280 + (((lane & 7) ^ (lane >> 3)) << 4);
  }
  asm volatile("s_waitcnt lgkmcnt(0)" ::"s"(kd));
  blds<0>(rsx, xsrc[0], rot, 128, mb); blds<8192>(rsx, xsrc[1], rot, 128, mb); blds<16384>(rsx, xsrc[2], rot, 128, mb);
  int ucol[2], ffj[2]; bool uf[2];
  if (ct < 8) { ucol[0] = 1280 + 160 * ct; ucol[1] = 2560 + 160 * ct; uf[0] = uf[1] = false; ffj[0] = ffj[1] = 0; }
  else if (ct < 16) { ucol[0] = 160 * (ct - 8); uf[0] = false; ffj[0] = 0; ffj[1] = ct - 8; ucol[1] = 160 * ffj[1]; uf[1] = true; }
  else { ffj[0] = 8 + 2 * (ct - 16); ffj[1] = ffj[0] + 1; ucol[0] = 160 * ffj[0]; ucol[1] = 160 * ffj[1]; uf[0] = uf[1] = true; }
  const int hg = ct & 7;
  unsigned wsrc[3]; __amdgpu_buffer_rsrc_t rsw[3]; int wstep[3];
#pragma unroll
  for (int i = 0; i < 3; ++i) {
    int pw = wave + 8 * i; pw = pw < 20 ? pw : 19;
    const int un = pw >= 10 ? 1 : 0;
    const int id = (pw - 10 * un) * 64 + lane;
    const int rho = id / 10; int c = id - rho * 10;
    const int cm = (un && ct == NCT - 1) ? 5 : 9;
    c = c < cm ? c : cm;
    const int kl = (rho & 32) + 8 * ((rho >> 2) & 3) + 4 * ((rho >> 4) & 1) + (rho & 3);
    const bool f = un ? uf[1] : uf[0];
    const int ld = f ? 3456 : 3840;
    wsrc[i] = kl * ld + c * 16 + (un ? ucol[1] : ucol[0]);
    rsw[i] = __builtin_amdgcn_make_buffer_rsrc((void*)(f ? w1 : wqkv), 0, 640 * ld, 0x00020000);
    wstep[i] = 64 * ld;
  }
#define KOF(s) ((s) + rot >= 10 ? (s) + rot - 10 : (s) + rot)
#define PIECE(s, p)                                                                                            \
  do {                                                                                                         \
    if ((p) < 3) blds<((s) % 3) * K1_STG + ((p) % 3) * 8192>(rsx, xsrc[(p) % 3], KOF(s), 128, mb);            \
    else if ((p) < 5 || wave < 4)                                                                              \
      blds<((s) % 3) * K1_STG + K1_XB + ((p) % 3) * 8192>(rsw[(p) % 3], wsrc[(p) % 3], KOF(s), wstep[(p) % 3], mb); \
  } while (0)
  PIECE(0, 3); PIECE(0, 4); PIECE(0, 5);
  BAR();
  PIECE(1, 0); PIECE(1, 1); PIECE(1, 2); PIECE(1, 3); PIECE(1, 4); PIECE(1, 5);
  if (wave >= 4) {
    int id = (wave - 4) * 64 + lane; id = id < 200 ? id : 199;
    glds(wphi + hg * 3200, id * 16, sb + K1_RAW + (wave - 4) * 1024);
  }
  BAR();
  PIECE(2, 0); PIECE(2, 1); PIECE(2, 2); PIECE(2, 3); PIECE(2, 4); PIECE(2, 5);
  if (tid < 16) *LDSP(unsigned, sb + K1_ZERO + tid * 4) = 0u;
  if (tid >= 64 && tid < 88) {
    const int i = tid - 64;
    *LDSP(u32x4, sb + (i >> 3) * K1_STG + K1_ONE + (i & 7) * 16) = u32x4{0x3F803F80u, 0x3F803F80u, 0x3F803F80u, 0x3F803F80u};
  }
  if (blockIdx.x == 0 && tid >= 128 && tid < 136) *(unsigned*)(ws + WS_CNT + (tid - 128) * 256) = 0u;
  const int rl_unit = ((ct - 8) * NRB + rb) * 4 + wave;
  const bool has_rl = (wave < 4 && ct >= 8 && rl_unit < 592);

  const bool ones_lane = (g == 3) && (fr == 15);
  unsigned xa[2][2], x2[2][2], wa[2][2], ga[2][3], ea[2];
#pragma unroll
  for (int ss = 0; ss < 2; ++ss) {
    const int sub = ss ^ u;
    const int r = 48 * g + fr;
    xa[0][ss] = sb + r * 128 + (((4 * sub + q4) ^ (r & 7)) << 4);
    x2[0][ss] = ones_lane ? sb + K1_ONE + q4 * 16 : xa[0][ss] + 4096;
    wa[0][ss] = sb + K1_XB + u * 10240 + (32 * sub + 4 * q4 + (fr >> 2)) * 160 + (fr & 3) * 8;
  }
#pragma unroll
  for (int a = 0; a < 3; ++a) ga[0][a] = fr == 0 ? sb + K1_ONE + q4 * 16 : xa[0][0] + a * 2048;
  ea[0] = sb + (48 * g + 16 * (fr < 3 ? fr : 0)) * 128 + ((4 * u + q4) << 4);
#pragma unroll
  for (int ss = 0; ss < 2; ++ss) {
    xa[1][ss] = xa[0][ss] + 2 * K1_STG; x2[1][ss] = x2[0][ss] + 2 * K1_STG; wa[1][ss] = wa[0][ss] + 2 * K1_STG;
    asm volatile("" : "+v"(xa[1][ss]), "+v"(x2[1][ss]), "+v"(wa[1][ss]));
  }
#pragma unroll
  for (int a = 0; a < 3; ++a) { ga[1][a] = ga[0][a] + 2 * K1_STG; asm volatile("" : "+v"(ga[1][a])); }
  ea[1] = ea[0] + 2 * K1_STG; asm volatile("" : "+v"(ea[1]));

  f32x4 acc[3][5], gram[3], gramE = f32x4{0.f, 0.f, 0.f, 0.f};
#pragma unroll
  for (int a = 0; a < 3; ++a) {
    gram[a] = f32x4{0.f, 0.f, 0.f, 0.f};
#pragma unroll
    for (int t = 0; t < 5; ++t) acc[a][t] = f32x4{0.f, 0.f, 0.f, 0.f};
  }
  bf16x8 fb[2][3], fg[3], fe;
  u32x2 wl[2][5], wh[2][5];
#define BS(s) (((s) % 3) == 2 ? 1 : 0)
#define BO(s) (((s) % 3) == 1 ? K1_STG : 0)
#define RB_(s, ss, a) fb[ss][a] = *LDSP(bf16x8, ((a) == 2 ? x2[BS(s)][ss] : xa[BS(s)][ss] + (a) * 2048) + BO(s))
#define RL_(s, ss, t) wl[ss][t] = tr_half(wa[BS(s)][ss] + BO(s) + (t) * 32)
#define RH_(s, ss, t) wh[ss][t] = tr_half(wa[BS(s)][ss] + BO(s) + (t) * 32 + 2560)
#define RG_(s, a) fg[a] = *LDSP(bf16x8, ga[BS(s)][a] + BO(s))
#define RE_(s) fe = *LDSP(bf16x8, ea[BS(s)] + BO(s))
#define MM(ss, t, a) acc[a][t] = mfma(cat(wl[ss][t], wh[ss][t]), fb[ss][a], acc[a][t])
#define GM(a) gram[a] = mfma(fg[a], fb[0][a], gram[a])
  if (wave < 4) WAITV(12); else WAITV(11);
  WAITL();
  BAR();
  STAMP(0, 1);
  RB_(0, 0, 0); RB_(0, 0, 1); RB_(0, 0, 2); RL_(0, 0, 0); RH_(0, 0, 0); RG_(0, 0); RG_(0, 1); RG_(0, 2); RE_(0);
  RL_(0, 0, 1); RH_(0, 0, 1); RL_(0, 0, 2); RH_(0, 0, 2); RL_(0, 0, 3); RH_(0, 0, 3); RL_(0, 0, 4); RH_(0, 0, 4);
  SB();
  auto stage = [&](auto SC) __attribute__((always_inline)) {
    constexpr int s = decltype(SC)::v;
    constexpr bool iss0 = (s >= 1 && s + 2 < 10), iss1 = (s + 3 < 10), nx = s < 9;
#define P0(c, st, p) do { SB(); if ((c) && u == 0) PIECE(st, p); SB(); } while (0)
#define P1(c, st, p) do { SB(); if ((c) && u == 1) PIECE(st, p); SB(); } while (0)
    MM(0, 0, 0); RB_(s, 1, 0); SB();
    MM(0, 0, 1); RB_(s, 1, 1); P0(iss0, s + 2, 2);
    MM(0, 0, 2); RB_(s, 1, 2); SB();
    GM(0); RL_(s, 1, 0); RH_(s, 1, 0); SB();
    GM(1); RL_(s, 1, 1); P1(iss0, s + 2, 2);
    GM(2); RH_(s, 1, 1); SB();
    gramE = mfma(fe, fe, gramE); RL_(s, 1, 2); SB();
    MM(0, 1, 0); RH_(s, 1, 2); P0(iss0, s + 2, 3);
    MM(0, 1, 1); RL_(s, 1, 3); SB();
    MM(0, 1, 2); RH_(s, 1, 3); SB();
    MM(0, 2, 0); RL_(s, 1, 4); P1(iss0, s + 2, 3);
    MM(0, 2, 1); RH_(s, 1, 4); SB();
    MM(0, 2, 2); SB();
    MM(0, 3, 0); P0(iss0, s + 2, 4);
    MM(0, 3, 1); MM(0, 3, 2); SB();
    MM(0, 4, 0); P1(iss0, s + 2, 4);
    MM(0, 4, 1); MM(0, 4, 2); P0(iss0, s + 2, 5);
    if (!nx) return;
    MM(1, 0, 0); MM(1, 0, 1); MM(1, 0, 2); SB();
    if (nx) {
      WAITL();
      if (s == 8) { if (has_rl) WAITV(5); else WAITV(0); }
      else if (u == 0) WAITV(6);
      else WAITV(5);
      BAR();
      if (s == 7 && has_rl) {
      const int cg = rl_unit / 74, ks = rl_unit - cg * 74;
      const char* src = (ks < 54 ? w2 + ks * 32 * 1280 : wo) + cg * 160;
#pragma unroll
      for (int i = 0; i < 5; ++i) {
        const int id = i * 64 + lane, rho = id / 10, c = id - rho * 10;
        int row = rho;
        if (ks >= 54) {
          const int ch = 4 * (ks - 54) + ((rho >> 2) & 3), P = ch / 5, r5 = ch - 5 * P, hi = rho >> 4, i4 = rho & 3;
          row = r5 < 4 ? 40 * P + 20 * (r5 >> 1) + 8 * (r5 & 1) + 4 * hi + i4 : 40 * P + 20 * hi + 16 + i4;
        }
        glds(src, row * 1280 + c * 16, sb + K1_RL + wave * 5120 + i * 1024);
      }
    }
      SB();
      if (s == 4) STAMP(0, 2);
    }
    MM(1, 1, 0); if (nx) { RB_(s + 1, 0, 0); RB_(s + 1, 0, 1); } SB();
    MM(1, 1, 1); if (nx) { RB_(s + 1, 0, 2); RL_(s + 1, 0, 0); } P0(iss1, s + 3, 0);
    MM(1, 1, 2); if (nx) { RH_(s + 1, 0, 0); RG_(s + 1, 0); } SB();
    MM(1, 2, 0); if (nx) { RG_(s + 1, 1); RG_(s + 1, 2); } SB();
    MM(1, 2, 1); if (nx) { RE_(s + 1); RL_(s + 1, 0, 1); } P1(iss1, s + 3, 0);
    MM(1, 2, 2); if (nx) { RH_(s + 1, 0, 1); RL_(s + 1, 0, 2); } SB();
    MM(1, 3, 0); if (nx) { RH_(s + 1, 0, 2); RL_(s + 1, 0, 3); } SB();
    MM(1, 3, 1); if (nx) { RH_(s + 1, 0, 3); RL_(s + 1, 0, 4); } P0(iss1, s + 3, 1);
    MM(1, 3, 2); if (nx) { RH_(s + 1, 0, 4); } SB();
    MM(1, 4, 0); SB();
    MM(1, 4, 1); P1(iss1, s + 3, 1);
    SB();
    MM(1, 4, 2); SB();
  };
  stage(IC<0>{}); stage(IC<1>{}); stage(IC<2>{}); stage(IC<3>{}); stage(IC<4>{});
  stage(IC<5>{}); stage(IC<6>{}); stage(IC<7>{}); stage(IC<8>{}); stage(IC<9>{});
  MM(1, 0, 2); MM(1, 1, 2); MM(1, 2, 2); MM(1, 3, 2); MM(1, 4, 2);
  MM(1, 0, 0); MM(1, 1, 0);
  {
    const int r3 = fr & 3;
    const bool diag = (q4 == (fr >> 2)) && fr != 0;
#pragma unroll
    for (int a = 0; a < 3; ++a) {
      const unsigned o = sb + K1_ST + (48 * g + 16 * a + fr) * 16 + u * 4;
      if (q4 == 0) *LDSP(float, o) = gram[a][0];
      const float dg = r3 == 0 ? gram[a][0] : r3 == 1 ? gram[a][1] : r3 == 2 ? gram[a][2] : gram[a][3];
      if (diag) *LDSP(float, o + 8) = dg;
    }
    const float de = r3 == 0 ? gramE[0] : r3 == 1 ? gramE[1] : gramE[2];
    if (lane < 3) *LDSP(float, sb + K1_ST + (48 * g + 16 * lane) * 16 + 8 + u * 4) = de;
  }
  SB();
  MM(1, 2, 0); MM(1, 3, 0); MM(1, 4, 0);
  if (ones_lane) {
#pragma unroll
    for (int t = 0; t < 5; ++t) *LDSP(f32x4, sb + K1_CS + (u * 80 + 16 * t + 4 * q4) * 4) = acc[2][t];
  }
  if (has_rl) {
    WAITV(0);
        const unsigned ra = sb + K1_RL + wave * 5120 + (4 * q4 + (fr >> 2)) * 160 + (fr & 3) * 8;
        const __amdgpu_buffer_rsrc_t rr = mkrs(ws, (unsigned)WS_WF + rl_unit * 5120);
#pragma unroll
        for (int t = 0; t < 5; ++t)
          __builtin_amdgcn_raw_buffer_store_b128(__builtin_bit_cast(u32x4, tr_frag(ra + t * 32, ra + t * 32 + 2560)), rr, lane * 16 + t * 1024, 0, RL_AUX);
        CFENCE();
      }
  SB();
  WAITL(); BAR();
  STAMP(0, 3);
  float rstd[3];
  {
    f32x4 cs[5], st[3];
#pragma unroll
    for (int a = 0; a < 3; ++a) st[a] = *LDSP(f32x4, sb + K1_ST + (48 * g + 16 * a + fr) * 16);
#pragma unroll
    for (int t = 0; t < 5; ++t) cs[t] = *LDSP(f32x4, sb + K1_CS + (u * 80 + 16 * t + 4 * q4) * 4);
    SB();
    MM(1, 0, 1); MM(1, 1, 1); MM(1, 2, 1); MM(1, 3, 1); MM(1, 4, 1);
    SB();
#pragma unroll
    for (int a = 0; a < 3; ++a) {
      const float nmean = (st[a][0] + st[a][1]) * (-1.f / 640.f);
      const float var = fmaxf(fmaf(st[a][2] + st[a][3], 1.f / 640.f, -nmean * nmean), 0.f);
      rstd[a] = __builtin_amdgcn_rsqf(var + 1e-5f);
#pragma unroll
      for (int t = 0; t < 5; ++t) {
        const f32x2 l = fma2(lo2(cs[t]), sp2(nmean), lo2(acc[a][t])), h = fma2(hi2(cs[t]), sp2(nmean), hi2(acc[a][t]));
        acc[a][t] = f32x4{l[0], l[1], h[0], h[1]};
      }
    }
  }
  const bool is_ffn = u ? uf[1] : uf[0];
  if (is_ffn) {
    const int j = u ? ffj[1] : ffj[0];
    const __amdgpu_buffer_rsrc_t rs = mkrs(ws, (unsigned)WS_H + ((5 * j) >> 1) * 1024);
    constexpr int hs = 0;
#pragma unroll
    for (int a = 0; a < 3; ++a) {
      const int slot = 48 * g + 16 * a + fr, tok = row0 + slot;
      const f32x2 rs2 = sp2(rstd[a]), nl2 = sp2(-LOG2E * rstd[a]);
      unsigned pk[10];
#pragma unroll
      for (int i = 0; i < 10; ++i) {
        const f32x2 z = (i & 1) ? hi2(acc[a][i >> 1]) : lo2(acc[a][i >> 1]);
        const f32x2 ar = z * nl2, y = z * rs2;
        const f32x2 d = f32x2{__builtin_amdgcn_exp2f(ar[0]), __builtin_amdgcn_exp2f(ar[1])} + sp2(1.f);
        pk[i] = pk2v(y * f32x2{__builtin_amdgcn_rcpf(d[0]), __builtin_amdgcn_rcpf(d[1])});
      }
      const unsigned vo = slot < nvalid ? (unsigned)(((tok >> 4) * 54 * 64 + q4 * 16 + (tok & 15)) * 16) : 0x7f000000u;
      if (!(j & 1)) {
        __builtin_amdgcn_raw_buffer_store_b128(u32x4{pk[0], pk[1], pk[2], pk[3]}, rs, vo, hs, ST_AUX);
        __builtin_amdgcn_raw_buffer_store_b128(u32x4{pk[4], pk[5], pk[6], pk[7]}, rs, vo + 1024, hs, ST_AUX);
        __builtin_amdgcn_raw_buffer_store_b64(u32x2{pk[8], pk[9]}, rs, vo + 2048, hs, ST_AUX);
      } else {
        __builtin_amdgcn_raw_buffer_store_b64(u32x2{pk[0], pk[1]}, rs, vo + 8, hs, ST_AUX);
        __builtin_amdgcn_raw_buffer_store_b128(u32x4{pk[2], pk[3], pk[4], pk[5]}, rs, vo + 1024, hs, ST_AUX);
        if (j != 21) __builtin_amdgcn_raw_buffer_store_b128(u32x4{pk[6], pk[7], pk[8], pk[9]}, rs, vo + 2048, hs, ST_AUX);
      }
    }
  } else if (u == 0) {
    __builtin_amdgcn_s_setprio(2);
    bf16x8 wf[5][3];
    {
      const int q = fr >> 2, p = fr & 3;
#pragma unroll
      for (int c = 0; c < 3; ++c) {
        const int ph0 = 32 * c + 4 * q4 + q, ph1 = ph0 + 16;
        const unsigned a0 = (ph0 / 20 == p) ? sb + K1_RAW + ph0 * 40 : sb + K1_ZERO;
        const unsigned a1 = (ph1 / 20 == p) ? sb + K1_RAW + ph1 * 40 : sb + K1_ZERO;
#pragma unroll
        for (int t = 0; t < 5; ++t) wf[t][c] = tr_frag(a0 + 8 * t, a1 + 8 * t);
      }
    }
    if (ct >= 8) {
#pragma unroll
    for (int a = 0; a < 3; ++a) {
      const int slot = 48 * g + 16 * a + fr, tok = row0 + slot;
      u32x4 qb[3];
      qb[0] = u32x4{pk2(acc[a][0][0], acc[a][0][1]), pk2(acc[a][0][2], acc[a][0][3]), pk2(acc[a][1][0], acc[a][1][1]), pk2(acc[a][1][2], acc[a][1][3])};
      qb[1] = u32x4{pk2(acc[a][2][0], acc[a][2][1]), pk2(acc[a][2][2], acc[a][2][3]), pk2(acc[a][3][0], acc[a][3][1]), pk2(acc[a][3][2], acc[a][3][3])};
      qb[2] = u32x4{pk2(acc[a][4][0], acc[a][4][1]), pk2(acc[a][4][2], acc[a][4][3]), 0u, 0u};
      f32x4 p[5];
#pragma unroll
      for (int t = 0; t < 5; ++t) {
        p[t] = f32x4{0.f, 0.f, 0.f, 0.f};
#pragma unroll
        for (int c = 0; c < 3; ++c) p[t] = mfma(wf[t][c], __builtin_bit_cast(bf16x8, qb[c]), p[t]);
      }
      const bool valid = slot < nvalid;
      {
        const f32x2 rs2 = sp2(rstd[a]);
        unsigned pb[10];
#pragma unroll
        for (int t = 0; t < 5; ++t) { pb[2 * t] = pk2v(lo2(p[t]) * rs2); pb[2 * t + 1] = pk2v(hi2(p[t]) * rs2); }
        const __amdgpu_buffer_rsrc_t rq = mkrs(ws, (unsigned)WS_PQ + hg * 4 * 81920);
        const unsigned vo = valid ? (unsigned)(q4 * 81920 + tok * 16) : 0x7f000000u;
        __builtin_amdgcn_raw_buffer_store_b128(u32x4{pb[0], pb[1], pb[2], pb[3]}, rq, vo, 0, ST_AUX);
        __builtin_amdgcn_raw_buffer_store_b128(u32x4{pb[4], pb[5], pb[6], pb[7]}, rq, vo + 32768, 0, ST_AUX);
        __builtin_amdgcn_raw_buffer_store_b64(u32x2{pb[8], pb[9]}, rq, vo + 65536 - tok * 8, 0, ST_AUX);
      }
    }
    } else {
    f32x4 pp[3][5];
#pragma unroll
    for (int a = 0; a < 3; ++a) {
      u32x4 qb[3];
      qb[0] = u32x4{pk2(acc[a][0][0], acc[a][0][1]), pk2(acc[a][0][2], acc[a][0][3]), pk2(acc[a][1][0], acc[a][1][1]), pk2(acc[a][1][2], acc[a][1][3])};
      qb[1] = u32x4{pk2(acc[a][2][0], acc[a][2][1]), pk2(acc[a][2][2], acc[a][2][3]), pk2(acc[a][3][0], acc[a][3][1]), pk2(acc[a][3][2], acc[a][3][3])};
      qb[2] = u32x4{pk2(acc[a][4][0], acc[a][4][1]), pk2(acc[a][4][2], acc[a][4][3]), 0u, 0u};
#pragma unroll
      for (int c = 0; c < 3; ++c)
#pragma unroll
        for (int t = 0; t < 5; ++t)
          pp[a][t] = mfma(wf[t][c], __builtin_bit_cast(bf16x8, qb[c]), c == 0 ? f32x4{0.f, 0.f, 0.f, 0.f} : pp[a][t]);
    }
#pragma unroll
    for (int a = 0; a < 3; ++a) {
      const int slot = 48 * g + 16 * a + fr;
      f32x4 (&p)[5] = pp[a];
      const bool valid = slot < nvalid;
      float mx = fmaxf(p[0][0], p[0][1]), mn = fminf(p[0][0], p[0][1]);
#pragma unroll
      for (int i = 1; i < 10; ++i) {
        const float v0 = p[i >> 1][(i & 1) * 2], v1 = p[i >> 1][(i & 1) * 2 + 1];
        mx = fmaxf(fmaxf(mx, v0), v1); mn = fminf(fminf(mn, v0), v1);
      }
      const float cc = LOG2E * rstd[a];
      const f32x2 c2 = sp2(cc), nc2 = sp2(-cc), bx = sp2(-mx * cc), bn = sp2(mn * cc);
      f32x2 ep[10], en[10], sp = sp2(0.f), sn = sp2(0.f);
#pragma unroll
      for (int i = 0; i < 10; ++i) {
        const f32x2 z = (i & 1) ? hi2(p[i >> 1]) : lo2(p[i >> 1]);
        const f32x2 ap = fma2(z, c2, bx), an = fma2(z, nc2, bn);
        ep[i] = f32x2{__builtin_amdgcn_exp2f(ap[0]), __builtin_amdgcn_exp2f(ap[1])};
        en[i] = f32x2{__builtin_amdgcn_exp2f(an[0]), __builtin_amdgcn_exp2f(an[1])};
        sp += ep[i]; sn += en[i];
      }
      const f32x2 rp = sp2(valid ? __builtin_amdgcn_rcpf(sp[0] + sp[1]) : 0.f), rn = sp2(valid ? __builtin_amdgcn_rcpf(sn[0] + sn[1]) : 0.f);
      unsigned ph[20];
#pragma unroll
      for (int i = 0; i < 10; ++i) { ph[i] = pk2v(ep[i] * rp); ph[10 + i] = pk2v(en[i] * rn); }
#pragma unroll
      for (int c = 0; c < 5; ++c)
        *LDSP(u32x4, sb + (64 * a + 16 * g + fr) * PKS + q4 * 80 + c * 16) = u32x4{ph[4 * c], ph[4 * c + 1], ph[4 * c + 2], ph[4 * c + 3]};
      SB(); WAITL(); BAR(); SB();
    }
    }
  } else {
#pragma unroll
    for (int a = 0; a < 3; ++a) {
      const int row = 64 * a + 16 * g + fr;
      const f32x2 rs2 = sp2(rstd[a]);
#pragma unroll
      for (int t = 0; t < 5; ++t) {
        const int ft = 16 * t + 4 * q4, hd = ft / 20, d = ft - hd * 20;
        *LDSP(u32x2, sb + K1_V + row * VS + hd * 64 + d * 2) = u32x2{pk2v(lo2(acc[a][t]) * rs2), pk2v(hi2(acc[a][t]) * rs2)};
      }
      *LDSP(u32x2, sb + K1_V + row * VS + q4 * 64 + 40) = u32x2{0x3F80u, 0u};
    }
    const int rr = 4 * q4 + (fr >> 2);
    const unsigned va = sb + K1_V + rr * VS + g * 64 + (fr & 3) * 8;
    const unsigned pa = sb + rr * PKS + g * 80 + (fr & 3) * 8;
    f32x4 kv[2][3];
#pragma unroll
    for (int c = 0; c < 3; ++c) { kv[0][c] = f32x4{0.f, 0.f, 0.f, 0.f}; kv[1][c] = f32x4{0.f, 0.f, 0.f, 0.f}; }
#pragma unroll
    for (int s = 0; s < 6; ++s) {
      if (!(s & 1)) { SB(); WAITL(); BAR(); SB(); }
      const bf16x8 av0 = tr_frag(va + s * 32 * VS, va + s * 32 * VS + 16 * VS);
      const bf16x8 av1 = tr_frag(va + s * 32 * VS + 32, va + s * 32 * VS + 16 * VS + 32);
#pragma unroll
      for (int c = 0; c < 3; ++c) {
        const bf16x8 bp = tr_frag(pa + s * 32 * PKS + c * 32, pa + s * 32 * PKS + 16 * PKS + c * 32);
        kv[0][c] = mfma(av0, bp, kv[0][c]);
        kv[1][c] = mfma(av1, bp, kv[1][c]);
      }
    }
    const __amdgpu_buffer_rsrc_t rs = mkrs(ws, (unsigned)WS_SL + (rb * 32 + 4 * hg + g) * 3840);
#pragma unroll
    for (int rt = 0; rt < 2; ++rt)
#pragma unroll
      for (int c = 0; c < 3; ++c) {
        const int f = 16 * c + fr, d = 16 * rt + 4 * q4;
        const unsigned vo = (f < 40 && d < 24) ? (unsigned)((f * 24 + d) * 4) : 0x7f000000u;
        __builtin_amdgcn_raw_buffer_store_b128(__builtin_bit_cast(u32x4, kv[rt][c]), rs, vo, 0, KV_AUX);
      }
  }
  STAMP(0, 4);
  STAMP(0, 5); STAMP_FLUSH(0);
}

constexpr int K2_D = 4;
#define OUT_AUX 0
#define CHK 9
#define PA_STEP 1
constexpr int K2_R1 = 0, K2_R2 = 61440, K2_KV = 122880, K2_CTR = K2_KV + 64 * 96, K2_PF = K2_CTR + 16, K2_LDS = K2_PF + 256;
typedef __attribute__((address_space(1))) unsigned gu32;

__global__ __launch_bounds__(256, 1) void k2_kernel(const char* __restrict__ x, char* __restrict__ ws,
                                                     char* __restrict__ out) {
  extern __shared__ __attribute__((aligned(16))) char smem[];
  const unsigned sb = (unsigned)(uintptr_t)smem;
  const int tid = threadIdx.x, lane = tid & 63;
  const int wave = __builtin_amdgcn_readfirstlane(tid >> 6);
  const int fr = lane & 15, q4 = lane >> 4;
  STAMP_DECL; STAMP(1, 0);
  if (wave == 3 && blockIdx.x < 16) {
    unsigned long long pc; int len;
    asm volatile("s_getpc_b64 %0\n.Lpf2_here:\n\ts_mov_b32 %1, .Lpf_end-.Lpf2_here" : "=s"(pc), "=s"(len));
    const __amdgpu_buffer_rsrc_t rc = __builtin_amdgcn_make_buffer_rsrc((void*)pc, 0, len - 4, 0x00020000);
#pragma unroll
    for (int i = 0; i < 2; ++i)
      asm volatile("s_mov_b32 m0, %2\n\ts_nop 0\n\tbuffer_load_dword %0, %1, 0 offen lds"
                   ::"v"(lane * 128 + i * 8192), "s"(rc), "s"(sb + K2_PF) : "memory");
  }
  const int head = (blockIdx.x & 7) * 4 + ((blockIdx.x >> 3) & 3), tq = blockIdx.x >> 5;
  int tg, cg;
  {
    const int bid = blockIdx.x, xcd = bid & 7, idx = bid >> 3;
    tg = (xcd >> 1) * 8 + (idx & 7); cg = (xcd & 1) * 4 + (idx >> 3);
  }
  const __amdgpu_buffer_rsrc_t rs = __builtin_amdgcn_make_buffer_rsrc((void*)ws, 0, (int)WS_CNT, 0x00020000);
  const unsigned l16 = lane * 16;
  const unsigned hsrc = (unsigned)WS_H + tg * 4 * 54 * 1024, wsrc = (unsigned)WS_WF + cg * 74 * 5120;
  bf16x8 ra[K2_D][4], rw[K2_D][5], ea[4], ew[5], ro[5][4];
  auto ldh = [&](bf16x8 (&d)[4], int ks) {
#pragma unroll
    for (int a = 0; a < 4; ++a)
      d[a] = __builtin_bit_cast(bf16x8, __builtin_amdgcn_raw_buffer_load_b128(rs, l16, hsrc + (((wave + a) & 3) * 54 + ks) * 1024, 0));
  };
  auto ldw = [&](bf16x8 (&d)[5], int ks) {
#pragma unroll
    for (int t = 0; t < 5; ++t)
      d[t] = __builtin_bit_cast(bf16x8, __builtin_amdgcn_raw_buffer_load_b128(rs, l16 + (t & 3) * 1024, wsrc + ks * 5120 + (t >> 2) * 4096, 0));
  };
  auto ldo = [&](bf16x8 (&d)[4], int i) {
#pragma unroll
    for (int a = 0; a < 4; ++a)
      d[a] = __builtin_bit_cast(bf16x8, __builtin_amdgcn_raw_buffer_load_b128(rs, l16, (unsigned)WS_O + ((tg * 4 + ((wave + a) & 3)) * 20 + 5 * wave + i) * 1024, 16));
  };
  const int eks = 52 + (wave & 1);
  u32x4 pq0, pq1; u32x2 pq2;
  {
    const unsigned pb = (unsigned)WS_PQ + head * 81920, tk = tq * 256 + tid;
    pq0 = __builtin_amdgcn_raw_buffer_load_b128(rs, tk * 16, pb, 0);
    pq1 = __builtin_amdgcn_raw_buffer_load_b128(rs, tk * 16, pb + 32768, 0);
    pq2 = __builtin_amdgcn_raw_buffer_load_b64(rs, tk * 8, pb + 65536, 0);
  }
  CFENCE();
  f32x4 sl[NRB];
  {
    const unsigned so = (tid < 240 ? tid : 239) * 16;
#pragma unroll
    for (int r = 0; r < NRB; ++r)
      sl[r] = __builtin_bit_cast(f32x4, __builtin_amdgcn_raw_buffer_load_b128(rs, so, (unsigned)WS_SL + (r * 32 + head) * 3840, 0));
  }
  CFENCE();
  ldh(ra[0], 13 * wave); ldw(rw[0], 13 * wave); CFENCE();
#pragma unroll
  for (int s = 1; s < K2_D; ++s) { ldh(ra[s], 13 * wave + s); ldw(rw[s], 13 * wave + s); CFENCE(); }
  const int tok = tg * 64 + wave * 16 + fr;
  const size_t ooff = (size_t)tok * 1280 + cg * 160 + q4 * 8;
  u32x2 xv[5];
  if (tid < 24 * 6) *LDSP(u32x4, sb + K2_KV + 40 * 96 + tid * 16) = u32x4{0u, 0u, 0u, 0u};
  if (tid == 255) *LDSP(unsigned, sb + K2_CTR) = 0u;
  SB();
  f32x4 acc[4][5];
  auto mm = [&](bf16x8 (&A)[4], bf16x8 (&W)[5], bool first) {
#pragma unroll
    for (int t = 0; t < 5; ++t)
#pragma unroll
      for (int a = 0; a < 4; ++a) acc[a][t] = mfma(W[t], A[a], first ? f32x4{0.f, 0.f, 0.f, 0.f} : acc[a][t]);
  };
  auto scatter = [&](unsigned reg) {
#pragma unroll
    for (int a = 1; a < 4; ++a) {
      const int slot = (((wave + a) & 3) * 3 + (a - 1)) * 5;
#pragma unroll
      for (int t = 0; t < 5; ++t) *LDSP(f32x4, reg + (slot + t) * 1024 + lane * 16) = acc[a][t];
    }
  };
  auto gather = [&](unsigned reg, f32x4 (&r)[5]) {
    WAITL(); BAR();
#pragma unroll
    for (int t = 0; t < 5; ++t) r[t] = acc[0][t];
#pragma unroll
    for (int j = 0; j < 3; ++j)
#pragma unroll
      for (int t = 0; t < 5; ++t) r[t] += *LDSP(f32x4, reg + ((wave * 3 + j) * 5 + t) * 1024 + lane * 16);
  };
  gu32* cnt_out = (gu32*)(ws + WS_CNT + tq * 256);
  gu32* cnt_in = (gu32*)(ws + WS_CNT + (tg >> 2) * 256);
  unsigned seen = 0;
  f32x4 gv[5];
  auto step = [&](auto JC) __attribute__((always_inline)) {
    constexpr int j = decltype(JC)::v, d = j % K2_D;
    if (j == 0) {
      {
        const unsigned w[10] = {pq0[0], pq0[1], pq0[2], pq0[3], pq1[0], pq1[1], pq1[2], pq1[3], pq2[0], pq2[1]};
        f32x2 z[10];
#pragma unroll
        for (int i = 0; i < 10; ++i) z[i] = f32x2{__builtin_bit_cast(float, w[i] << 16), __builtin_bit_cast(float, w[i] & 0xffff0000u)};
        float mx = max3(z[0][0], z[0][1], z[0][1]), mn = min3(z[0][0], z[0][1], z[0][1]);
#pragma unroll
        for (int i = 1; i < 10; ++i) { mx = max3(mx, z[i][0], z[i][1]); mn = min3(mn, z[i][0], z[i][1]); }
        const f32x2 c2 = sp2(LOG2E), nc2 = sp2(-LOG2E), bx = sp2(-mx * LOG2E), bn = sp2(mn * LOG2E);
        f32x2 ep[10], en[10], sp = sp2(0.f), sn = sp2(0.f);
#pragma unroll
        for (int i = 0; i < 10; ++i) {
          const f32x2 ap = fma2(z[i], c2, bx), an = fma2(z[i], nc2, bn);
          ep[i] = f32x2{__builtin_amdgcn_exp2f(ap[0]), __builtin_amdgcn_exp2f(ap[1])};
          en[i] = f32x2{__builtin_amdgcn_exp2f(an[0]), __builtin_amdgcn_exp2f(an[1])};
          sp += ep[i]; sn += en[i];
        }
        const f32x2 rp = sp2(__builtin_amdgcn_rcpf(sp[0] + sp[1])), rn = sp2(__builtin_amdgcn_rcpf(sn[0] + sn[1]));
        unsigned ph[20];
#pragma unroll
        for (int i = 0; i < 10; ++i) { ph[i] = pk2v(ep[i] * rp); ph[10 + i] = pk2v(en[i] * rn); }
#pragma unroll
        for (int c = 0; c < 5; ++c)
          *LDSP(u32x4, sb + K2_R2 + wave * 5120 + c * 1024 + lane * 16) = u32x4{ph[4 * c], ph[4 * c + 1], ph[4 * c + 2], ph[4 * c + 3]};
      }
      SB();
      f32x4 s = sl[0];
#pragma unroll
      for (int r = 1; r < NRB; ++r) s += sl[r];
      const int f = tid / 6, c4 = tid - f * 6;
      const unsigned row = sb + K2_KV + f * 96;
      const u32x2 v = u32x2{pk2(s[0], s[1]), pk2(s[2], s[3])};
      if (tid < 240) {
        if (c4 < 5) *LDSP(u32x2, row + (c4 & 1) * 32 + (c4 >> 1) * 8) = v;
        else {
#pragma unroll
          for (int p = 0; p < 4; ++p) *LDSP(u32x2, row + 64 + p * 8) = v;
        }
      }
      STAMP(1, 1);
      SB();
    }
    mm(ra[d], rw[d], j == 0);
    SB();
    if (j + K2_D < 13) { ldh(ra[d], 13 * wave + j + K2_D); ldw(rw[d], 13 * wave + j + K2_D); }
    else if (j + K2_D < 18) ldw(rw[d], 54 + 5 * wave + (j + K2_D - 13));
    if (j == PA_STEP) {
      CFENCE();
      WAITL(); BAR();
      bf16x8 A[3][2];
      const unsigned aa = sb + K2_KV + (8 * q4 + (fr >> 2)) * 96 + (fr & 3) * 8;
#pragma unroll
      for (int rt = 0; rt < 3; ++rt)
#pragma unroll
        for (int s = 0; s < 2; ++s) A[rt][s] = tr_frag(aa + s * 32 * 96 + rt * 32, aa + s * 32 * 96 + 4 * 96 + rt * 32);
      const int P = head >> 1, hb = head & 1;
      const int chunk = q4 < 2 ? 5 * P + 2 * hb + q4 : 5 * P + 4;
      const unsigned ob = (unsigned)(chunk * 256 + fr * 16 + (q4 < 2 ? 0 : hb * 8));
      const unsigned os = (unsigned)WS_O + (tq * 16 + wave * 4) * 20480;
#pragma unroll
      for (int i = 0; i < 4; ++i) {
        f32x4 c[3];
        const bf16x8 b0 = *LDSP(bf16x8, sb + K2_R2 + wave * 5120 + q4 * 1024 + (16 * i + fr) * 16);
        const bf16x8 b1 = *LDSP(bf16x8, sb + K2_R2 + wave * 5120 + 4096 + (16 * i + fr) * 16);
#pragma unroll
        for (int rt = 0; rt < 3; ++rt) {
          c[rt] = mfma(A[rt][0], b0, f32x4{0.f, 0.f, 0.f, 0.f});
          c[rt] = mfma(A[rt][1], b1, c[rt]);
        }
        const float rd = __builtin_amdgcn_rcpf(c[2][0] + 1e-6f);
        const u32x4 v = u32x4{pk2(c[0][0] * rd, c[0][1] * rd), pk2(c[0][2] * rd, c[0][3] * rd),
                              pk2(c[1][0] * rd, c[1][1] * rd), pk2(c[1][2] * rd, c[1][3] * rd)};
        const __amdgpu_buffer_rsrc_t ro = mkrs(ws, os + i * 20480);
        if (q4 < 2) __builtin_amdgcn_raw_buffer_store_b128(v, ro, ob, 0, 16);
        else if (q4 == 2) __builtin_amdgcn_raw_buffer_store_b64(u32x2{v[0], v[1]}, ro, ob, 0, 16);
      }
      CFENCE();
      STAMP(1, 2);
    }
    if (j == PA_STEP + 3) {
      WAITV(27);
      if (lane == 0) {
        const unsigned old = __hip_atomic_fetch_add(LDSP(unsigned, sb + K2_CTR), 1u, __ATOMIC_RELAXED, __HIP_MEMORY_SCOPE_WORKGROUP);
        if (old == 3u) __hip_atomic_fetch_add(cnt_out, 1u, __ATOMIC_RELAXED, __HIP_MEMORY_SCOPE_AGENT);
      }
      CFENCE();
    }
    if (j == 5 && wave < 2) { ldh(ea, eks); ldw(ew, eks); }
    if (j == CHK - 2) seen = __hip_atomic_load(cnt_in, __ATOMIC_RELAXED, __HIP_MEMORY_SCOPE_AGENT);
    if (j == 9 && wave < 2) mm(ea, ew, false);
    if (j == CHK) {
      STAMP(1, 3);
      unsigned v = __builtin_amdgcn_readfirstlane(seen);
      for (int spin = 0; v < 32u && spin < (1 << 22); ++spin) {
        __builtin_amdgcn_s_sleep(2);
        v = __builtin_amdgcn_readfirstlane(__hip_atomic_load(cnt_in, __ATOMIC_RELAXED, __HIP_MEMORY_SCOPE_AGENT));
      }
      __builtin_amdgcn_fence(__ATOMIC_ACQUIRE, "wavefront");
      CFENCE();
      STAMP(1, 4);
      ldo(ro[0], 0); ldo(ro[1], 1);
    }
    if (j == CHK + 1) { ldo(ro[2], 2); ldo(ro[3], 3); }
    if (j == CHK + 2) ldo(ro[4], 4);
    if (j == 12) {
#pragma unroll
      for (int t = 0; t < 5; ++t) xv[t] = *(const u32x2*)(x + ooff + t * 32);
    }
    SB();
  };
  step(IC<0>{}); step(IC<1>{}); step(IC<2>{}); step(IC<3>{}); step(IC<4>{}); step(IC<5>{});
  step(IC<6>{}); step(IC<7>{}); step(IC<8>{}); step(IC<9>{}); step(IC<10>{}); step(IC<11>{});
  step(IC<12>{});
  STAMP(1, 5);
  f32x4 ac2[4][5], g1[5], g2[5], g3[5];
#define M2(i, t) _Pragma("unroll") for (int a = 0; a < 4; ++a) \
    ac2[a][t] = mfma(rw[(13 + (i)) % K2_D][t], ro[i][a], (i) == 0 ? f32x4{0.f, 0.f, 0.f, 0.f} : ac2[a][t])
#pragma unroll
  for (int t = 0; t < 5; ++t) {
    M2(0, t);
#pragma unroll
    for (int a = 1; a < 4; ++a)
      *LDSP(f32x4, sb + K2_R1 + ((((wave + a) & 3) * 3 + (a - 1)) * 5 + t) * 1024 + lane * 16) = acc[a][t];
    SB();
  }
  ldw(rw[13 % K2_D], 54 + 5 * wave + 4);
  SB(); WAITL(); BAR(); SB();
#pragma unroll
  for (int t = 0; t < 5; ++t) {
    g1[t] = *LDSP(f32x4, sb + K2_R1 + ((wave * 3 + 0) * 5 + t) * 1024 + lane * 16);
    g2[t] = *LDSP(f32x4, sb + K2_R1 + ((wave * 3 + 1) * 5 + t) * 1024 + lane * 16);
    g3[t] = *LDSP(f32x4, sb + K2_R1 + ((wave * 3 + 2) * 5 + t) * 1024 + lane * 16);
    M2(1, t);
    SB();
  }
#pragma unroll
  for (int t = 0; t < 5; ++t) {
    M2(2, t);
    gv[t] = ((acc[0][t] + g1[t]) + g2[t]) + g3[t];
    const f32x4 ar = gv[t] * -LOG2E;
    g1[t] = f32x4{__builtin_amdgcn_exp2f(ar[0]), __builtin_amdgcn_exp2f(ar[1]), ar[2], ar[3]};
    SB();
  }
#pragma unroll
  for (int t = 0; t < 5; ++t) {
    M2(3, t);
    const f32x4 dd = f32x4{g1[t][0], g1[t][1], __builtin_amdgcn_exp2f(g1[t][2]), __builtin_amdgcn_exp2f(g1[t][3])} + 1.f;
    g1[t] = f32x4{__builtin_amdgcn_rcpf(dd[0]), dd[1], dd[2], dd[3]};
    SB();
  }
#pragma unroll
  for (int t = 0; t < 5; ++t) {
    M2(4, t);
    gv[t] *= f32x4{g1[t][0], __builtin_amdgcn_rcpf(g1[t][1]), __builtin_amdgcn_rcpf(g1[t][2]), __builtin_amdgcn_rcpf(g1[t][3])};
    SB();
  }
#pragma unroll
  for (int a = 0; a < 4; ++a)
#pragma unroll
    for (int t = 0; t < 5; ++t) acc[a][t] = ac2[a][t];
  STAMP(1, 6);
  f32x4 sv[5];
  const __amdgpu_buffer_rsrc_t rso = __builtin_amdgcn_make_buffer_rsrc((void*)out, 0, S_ * D_ * 2, 0x00020000);
  scatter(sb + K2_R2);
  gather(sb + K2_R2, sv);
#pragma unroll
  for (int t = 0; t < 5; ++t) {
    float o[4];
#pragma unroll
    for (int r = 0; r < 4; ++r) {
      const unsigned xw = xv[t][r >> 1];
      const float xf = __builtin_bit_cast(float, (r & 1) ? (xw & 0xffff0000u) : (xw << 16));
      o[r] = fmaf(sv[t][r], gv[t][r], xf);
    }
    __builtin_amdgcn_raw_buffer_store_b64(u32x2{pk2(o[0], o[1]), pk2(o[2], o[3])}, rso, (unsigned)ooff + t * 32, 0, OUT_AUX);
  }
  asm volatile(".Lpf_end:" ::: "memory");
  STAMP(1, 7); STAMP_FLUSH(1);
}

extern "C" void kernel_launch(void* const* d_in, const int* in_sizes, int n_in, void* d_out, int out_size, void* d_ws,
                              size_t ws_size, hipStream_t stream) {
  const char* x = (const char*)d_in[0];
  const char* wqkv = (const char*)d_in[1];
  const char* wphi = (const char*)d_in[2];
  const char* wo = (const char*)d_in[3];
  const char* w1 = (const char*)d_in[5];
  const char* w2 = (const char*)d_in[6];
  char* ws = (char*)d_ws;
  (void)hipFuncSetAttribute((const void*)k1_kernel, hipFuncAttributeMaxDynamicSharedMemorySize, K1_LDS);
  (void)hipFuncSetAttribute((const void*)k2_kernel, hipFuncAttributeMaxDynamicSharedMemorySize, K2_LDS);
  k1_kernel<<<NRB * NCT, 512, K1_LDS, stream>>>(x, wqkv, wphi, w1, wo, w2, ws);
  k2_kernel<<<256, 256, K2_LDS, stream>>>(x, ws, (char*)d_out);
}
```
